```python
import torch

def _softmax(x, axis=-1):
    return torch.softmax(x, dim=axis)
def _gelu(x):
    return torch.nn.functional.gelu(x, approximate="tanh")
def _silu(x):
    return torch.nn.functional.silu(x)
def _sigmoid(x):
    return torch.sigmoid(x)
def _layernorm(x, axis=-1, eps=1e-5):
    m = x.mean(dim=axis, keepdim=True)
    v = x.var(dim=axis, keepdim=True, unbiased=False)
    return (x - m) / torch.sqrt(v + eps)
def _scatter_add(tgt, idx, src, axis=0):
    return tgt.clone().index_add_(axis, idx, src)
def _segment_sum(src, seg, n):
    out = torch.zeros((n,) + src.shape[1:], dtype=src.dtype)
    return out.index_add_(0, seg, src)
def _pad(x, pw):
    flat = tuple(v for pair in reversed(pw) for v in pair)
    return torch.nn.functional.pad(x, flat)

kernel_name = 'gated_mix__mha_block_cached__gqa_block_cached__decode'

def setup_inputs():

    _w = (lambda a: (lambda t: t.to(torch.bfloat16) if t.is_floating_point() else t)(torch.as_tensor(a)))
    def _A_setup():
        B, Sq, D = 1, 1, 448
        H, Sk, Dh = 16, 8192, 256
        return {
            "x": _w((lambda *s: torch.randn(*s, dtype=torch.bfloat16) * 0.05)(B, Sq, D)),
            "k_cache": _w((lambda *s: torch.randn(*s, dtype=torch.bfloat16) * 3.2)(B, H, Sk, Dh)),
            "v_cache": _w((lambda *s: torch.randn(*s, dtype=torch.bfloat16) * 0.1)(B, H, Sk, Dh)),
            "wq": _w((lambda *s: torch.randn(*s, dtype=torch.bfloat16) * 3.2)(D, H * Dh) * (D ** -0.5)),
            "wo": _w((lambda *s: torch.randn(*s, dtype=torch.bfloat16) * 0.1)(H * Dh, D) * ((H * Dh) ** -0.5)),
        }
    _A_in = _A_setup()

    def _B_setup():
        B, Sq, D = 1, 1, 448
        Hq, Hkv, Sk, Dh = 16, 8, 8192, 256
        assert Hq % Hkv == 0, (Hq, Hkv)
        return {
            "x": _w((lambda *s: torch.randn(*s, dtype=torch.bfloat16) * 0.05)(B, Sq, D)),
            "k_cache": _w((lambda *s: torch.randn(*s, dtype=torch.bfloat16) * 3.2)(B, Hkv, Sk, Dh)),
            "v_cache": _w((lambda *s: torch.randn(*s, dtype=torch.bfloat16) * 0.05)(B, Hkv, Sk, Dh)),
            "wq": _w((lambda *s: torch.randn(*s, dtype=torch.bfloat16) * 3.2)(D, Hq * Dh) * (D ** -0.5)),
            "wo": _w((lambda *s: torch.randn(*s, dtype=torch.bfloat16) * 0.05)(Hq * Dh, D) * ((Hq * Dh) ** -0.5)),
        }
    _B_in = _B_setup()

    return {
        "A_x": _A_in["x"],
        "A_k_cache": _A_in["k_cache"],
        "A_v_cache": _A_in["v_cache"],
        "A_wq": _A_in["wq"],
        "A_wo": _A_in["wo"],
        "B_x": _B_in["x"],
        "B_k_cache": _B_in["k_cache"],
        "B_v_cache": _B_in["v_cache"],
        "B_wq": _B_in["wq"],
        "B_wo": _B_in["wo"],
        "gate_w": _w((lambda *s: torch.randn(*s, dtype=torch.bfloat16) * 0.1)(448) * 16.0),
    }

def reference(A_x, A_k_cache, A_v_cache, A_wq, A_wo, B_x, B_k_cache, B_v_cache, B_wq, B_wo, gate_w):
    def _A_ref(x, k_cache, v_cache, wq, wo):
        B, Sq, D = x.shape
        H, Dh = 16, 256
        q = (lambda x, *d: x.permute(*d))((x @ wq).reshape(B, Sq, H, Dh), 0, 2, 1, 3)
        scores = torch.einsum("bhqd,bhkd->bhqk", q, k_cache) * (Dh ** -0.5)
        p = _softmax(scores, axis=-1)
        o = torch.einsum("bhqk,bhkd->bhqd", p, v_cache)
        return (lambda x, *d: x.permute(*d))(o, 0, 2, 1, 3).reshape(B, Sq, H * Dh) @ wo

    def _B_ref(x, k_cache, v_cache, wq, wo):
        B, Sq, D = x.shape
        Hq, Dh = 16, 256
        Hkv = k_cache.shape[1]
        rep = Hq // Hkv
        q = (lambda x, *d: x.permute(*d))((x @ wq).reshape(B, Sq, Hq, Dh), 0, 2, 1, 3)
        k = (lambda x, n, axis: torch.repeat_interleave(x, n, dim=axis))(k_cache, rep, axis=1)
        v = (lambda x, n, axis: torch.repeat_interleave(x, n, dim=axis))(v_cache, rep, axis=1)
        scores = torch.einsum("bhqd,bhkd->bhqk", q, k) * (Dh ** -0.5)
        p = _softmax(scores, axis=-1)
        o = torch.einsum("bhqk,bhkd->bhqd", p, v)
        return (lambda x, *d: x.permute(*d))(o, 0, 2, 1, 3).reshape(B, Sq, Hq * Dh) @ wo

    x = A_x
    g = _sigmoid((x * gate_w).sum(-1, keepdims=True))
    a = _A_ref(x, A_k_cache, A_v_cache, A_wq, A_wo)
    b = _B_ref(x, B_k_cache, B_v_cache, B_wq, B_wo)
    return g * a + (1 - g) * b
```

```cpp
#include <hip/hip_runtime.h>
#include <hip/hip_bf16.h>
#include <stdint.h>

typedef unsigned u32x4 __attribute__((ext_vector_type(4)));
typedef float f32x4 __attribute__((ext_vector_type(4)));
typedef __bf16 bf16x2 __attribute__((ext_vector_type(2)));
typedef unsigned short u16;

constexpr int D = 448, DH = 256, SK = 8192, NQ = 4096;

__device__ __forceinline__ float dot2(unsigned a, unsigned b, float acc) {
  return __builtin_amdgcn_fdot2_f32_bf16(__builtin_bit_cast(bf16x2, a), __builtin_bit_cast(bf16x2, b), acc, false);
}
__device__ __forceinline__ float bflo(unsigned u) { return __builtin_bit_cast(float, u << 16); }
__device__ __forceinline__ float bfhi(unsigned u) { return __builtin_bit_cast(float, u & 0xffff0000u); }
__device__ __forceinline__ unsigned cvtpk(float lo, float hi) {
  unsigned r; asm("v_cvt_pk_bf16_f32 %0, %1, %2" : "=v"(r) : "v"(lo), "v"(hi)); return r;
}
template <int CTRL> __device__ __forceinline__ float dpp(float x) {
  return __builtin_bit_cast(float, __builtin_amdgcn_mov_dpp(__builtin_bit_cast(int, x), CTRL, 0xf, 0xf, true));
}
constexpr int XOR1 = 0xB1, XOR2 = 0x4E, XOR7 = 0x141, XOR8 = 0x128;

__global__ void __launch_bounds__(256) k_qproj(const u16* __restrict__ x, const u16* __restrict__ wqA, const u16* __restrict__ wqB, u16* __restrict__ qout) {
  __shared__ float red[64][33];
  const int b = blockIdx.x;
  const u16* wq = (b < 128) ? wqA : wqB;
  const int col0 = (b & 127) * 32;
  const int tid = threadIdx.x, c = tid & 3, r = tid >> 2;
  float acc[8];
#pragma unroll
  for (int j = 0; j < 8; ++j) acc[j] = 0.f;
  u32x4 w[7]; float xv[7];
#pragma unroll
  for (int i = 0; i < 7; ++i) { const int row = r + 64 * i; w[i] = *(const u32x4*)(wq + (size_t)row * NQ + col0 + 8 * c); xv[i] = __builtin_bit_cast(float, (unsigned)x[row] << 16); }
#pragma unroll
  for (int i = 0; i < 7; ++i)
#pragma unroll
    for (int k = 0; k < 4; ++k) { acc[2 * k] = fmaf(bflo(w[i][k]), xv[i], acc[2 * k]); acc[2 * k + 1] = fmaf(bfhi(w[i][k]), xv[i], acc[2 * k + 1]); }
#pragma unroll
  for (int j = 0; j < 8; ++j) red[r][8 * c + j] = acc[j];
  __syncthreads();
  if (tid < 32) {
    float s = 0.f;
    for (int i = 0; i < 64; ++i) s += red[i][tid];
    qout[b * 32 + tid] = (u16)(cvtpk(s, s) & 0xffff);
  }
}

template <int NH>
__device__ __forceinline__ void wave_attn(const u16* __restrict__ kp, const u16* __restrict__ vp, const u16* __restrict__ q, int lane,
                                          float* __restrict__ sp  , float (&acc)[NH][8], float (&mo)[NH], float (&lo)[NH]) {
  const int c = lane & 31, hf = lane >> 5;
  u32x4 qv[NH];
#pragma unroll
  for (int h = 0; h < NH; ++h) qv[h] = *(const u32x4*)(q + h * DH + 8 * c);
  const u32x4* kq = (const u32x4*)kp + lane;
  const u32x4* vq = (const u32x4*)vp + lane;
  constexpr int RING = 20;
  u32x4 buf[RING];
#pragma unroll
  for (int i = 0; i < RING; ++i) buf[i] = __builtin_nontemporal_load(kq + 64 * i);
  __builtin_amdgcn_sched_barrier(0);
  float sc[NH][2];
#pragma unroll
  for (int half = 0; half < 2; ++half) {
    float xx[NH][16];
#pragma unroll
    for (int i = 0; i < 16; ++i) {
      const int n = half * 16 + i;
      const u32x4 kk = buf[n % RING];
#pragma unroll
      for (int h = 0; h < NH; ++h) { float d = dot2(kk[0], qv[h][0], 0.f); d = dot2(kk[1], qv[h][1], d); d = dot2(kk[2], qv[h][2], d); d = dot2(kk[3], qv[h][3], d); xx[h][i] = d; }
      buf[n % RING] = (n + RING < 32) ? __builtin_nontemporal_load(kq + 64 * (n + RING)) : __builtin_nontemporal_load(vq + 64 * (n + RING - 32));
      __builtin_amdgcn_sched_barrier(0);
    }
#pragma unroll
    for (int h = 0; h < NH; ++h) {
#define TR_STEP(HALF, CTRL, BIT) _Pragma("unroll") for (int u = 0; u < (HALF); ++u) { \
      const float lo_ = xx[h][u], hi_ = xx[h][u + (HALF)]; const float t1_ = lo_ + dpp<CTRL>(lo_), t2_ = hi_ + dpp<CTRL>(hi_); \
      xx[h][u] = (lane & (BIT)) ? t2_ : t1_; }
      TR_STEP(8, XOR8, 8) TR_STEP(4, XOR7, 4) TR_STEP(2, XOR2, 2) TR_STEP(1, XOR1, 1)
#undef TR_STEP
      auto s = __builtin_amdgcn_permlane16_swap(__float_as_uint(xx[h][0]), __float_as_uint(xx[h][0]), false, false);
      sc[h][half] = (__uint_as_float(s[0]) + __uint_as_float(s[1])) * (0.0625f * 1.4426950408889634f);
    }
  }
#pragma unroll
  for (int h = 0; h < NH; ++h) {
    float m = fmaxf(sc[h][0], sc[h][1]);
    m = fmaxf(m, dpp<XOR1>(m)); m = fmaxf(m, dpp<XOR2>(m)); m = fmaxf(m, dpp<XOR7>(m)); m = fmaxf(m, dpp<XOR8>(m));
    { auto t = __builtin_amdgcn_permlane32_swap(__float_as_uint(m), __float_as_uint(m), false, false); m = fmaxf(__uint_as_float(t[0]), __uint_as_float(t[1])); }
    const float p0 = __builtin_amdgcn_exp2f(sc[h][0] - m), p1 = __builtin_amdgcn_exp2f(sc[h][1] - m);
    float l = p0 + p1;
    l += dpp<XOR1>(l); l += dpp<XOR2>(l); l += dpp<XOR7>(l); l += dpp<XOR8>(l);
    { auto t = __builtin_amdgcn_permlane32_swap(__float_as_uint(l), __float_as_uint(l), false, false); l = __uint_as_float(t[0]) + __uint_as_float(t[1]); }
    mo[h] = m; lo[h] = l;
    const float pv = (lane & 16) ? p1 : p0;
    sp[h * 64 + hf * 32 + (lane & 31)] = pv;
  }
  asm volatile("s_waitcnt lgkmcnt(0)" ::: "memory");
#pragma unroll
  for (int h = 0; h < NH; ++h)
#pragma unroll
    for (int k = 0; k < 8; ++k) acc[h][k] = 0.f;
#pragma unroll
  for (int i4 = 0; i4 < 8; ++i4) {
    f32x4 pr[NH];
#pragma unroll
    for (int h = 0; h < NH; ++h) pr[h] = *(const f32x4*)(sp + h * 64 + hf * 32 + 4 * i4);
#pragma unroll
    for (int u = 0; u < 4; ++u) {
      const int n = 32 + 4 * i4 + u;
      const u32x4 vv = buf[n % RING];
      float vf[8];
#pragma unroll
      for (int k = 0; k < 4; ++k) { vf[2 * k] = bflo(vv[k]); vf[2 * k + 1] = bfhi(vv[k]); }
#pragma unroll
      for (int h = 0; h < NH; ++h)
#pragma unroll
        for (int k = 0; k < 8; ++k) acc[h][k] = fmaf(pr[h][u], vf[k], acc[h][k]);
      if (n + RING < 64) buf[n % RING] = __builtin_nontemporal_load(vq + 64 * (n + RING - 32));
      __builtin_amdgcn_sched_barrier(0);
    }
  }
#pragma unroll
  for (int h = 0; h < NH; ++h)
#pragma unroll
    for (int k = 0; k < 8; ++k) { auto t = __builtin_amdgcn_permlane32_swap(__float_as_uint(acc[h][k]), __float_as_uint(acc[h][k]), false, false); acc[h][k] = __uint_as_float(t[0]) + __uint_as_float(t[1]); }
}

__global__ void __launch_bounds__(768, 3) k_attn(const u16* __restrict__ kA, const u16* __restrict__ vA, const u16* __restrict__ kB, const u16* __restrict__ vB,
                                                 const u16* __restrict__ q, float* __restrict__ po, float* __restrict__ pml) {
  __shared__ __attribute__((aligned(16))) float sp[12][128];
  __shared__ __attribute__((aligned(16))) float mrg[16][DH];
  __shared__ float mlm[16], mll[16];
  const int b = blockIdx.x, j = b & 7, idx = b >> 3;
  const int tid = threadIdx.x, lane = tid & 63, w = __builtin_amdgcn_readfirstlane(tid >> 6), ty = w >> 2, w4 = w & 3;
  const int row0 = idx * 256 + w4 * 64;
  const int c = lane & 31;
  if (ty < 2) {
    const int h = 2 * j + ty;
    const size_t off = ((size_t)h * SK + row0) * DH;
    float acc[1][8], m[1], l[1];
    wave_attn<1>(kA + off, vA + off, q + h * DH, lane, sp[w], acc, m, l);
    const int slot = ty * 4 + w4;
    if (lane < 32) { *(f32x4*)&mrg[slot][8 * c] = f32x4{acc[0][0], acc[0][1], acc[0][2], acc[0][3]}; *(f32x4*)&mrg[slot][8 * c + 4] = f32x4{acc[0][4], acc[0][5], acc[0][6], acc[0][7]}; }
    if (lane == 0) { mlm[slot] = m[0]; mll[slot] = l[0]; }
  } else {
    const size_t off = ((size_t)j * SK + row0) * DH;
    float acc[2][8], m[2], l[2];
    wave_attn<2>(kB + off, vB + off, q + NQ + 2 * j * DH, lane, sp[w], acc, m, l);
#pragma unroll
    for (int h = 0; h < 2; ++h) {
      const int slot = (2 + h) * 4 + w4;
      if (lane < 32) { *(f32x4*)&mrg[slot][8 * c] = f32x4{acc[h][0], acc[h][1], acc[h][2], acc[h][3]}; *(f32x4*)&mrg[slot][8 * c + 4] = f32x4{acc[h][4], acc[h][5], acc[h][6], acc[h][7]}; }
      if (lane == 0) { mlm[slot] = m[h]; mll[slot] = l[h]; }
    }
  }
  __syncthreads();
  if (tid < 256) {
    const int qh = tid >> 6, d4 = (tid & 63) * 4;
    float M = fmaxf(fmaxf(mlm[qh * 4], mlm[qh * 4 + 1]), fmaxf(mlm[qh * 4 + 2], mlm[qh * 4 + 3]));
    float L = 0.f; f32x4 o = {0.f, 0.f, 0.f, 0.f};
#pragma unroll
    for (int u = 0; u < 4; ++u) { const float wt = __builtin_amdgcn_exp2f(mlm[qh * 4 + u] - M); L = fmaf(wt, mll[qh * 4 + u], L); o += wt * *(const f32x4*)&mrg[qh * 4 + u][d4]; }
    const int gq = (qh < 2) ? (2 * j + qh) : (16 + 2 * j + (qh - 2));
    *(f32x4*)(po + ((size_t)gq * 32 + idx) * DH + d4) = o;
    if ((tid & 63) == 0) { pml[(gq * 32 + idx) * 2] = M; pml[(gq * 32 + idx) * 2 + 1] = L; }
  }
}

__global__ void __launch_bounds__(256) k_comb(const float* __restrict__ po, const float* __restrict__ pml, const u16* __restrict__ woA, const u16* __restrict__ woB, float* __restrict__ y) {
  __shared__ float red[8][32];
  __shared__ float os[32];
  __shared__ __attribute__((aligned(16))) float yr[4][448];
  const int b = blockIdx.x, gq = b >> 3, sl = b & 7, tid = threadIdx.x;
  const u16* wo = (gq < 16 ? woA + (size_t)gq * DH * D : woB + (size_t)(gq - 16) * DH * D) + (size_t)sl * 32 * D;
  const int rg = tid / 56, cc = tid % 56;
  u32x4 wv[8];
  if (tid < 224) {
#pragma unroll
    for (int i = 0; i < 8; ++i) wv[i] = *(const u32x4*)(wo + (size_t)(rg * 8 + i) * D + 8 * cc);
  }
  float M = -INFINITY;
  for (int s = 0; s < 32; ++s) M = fmaxf(M, pml[(gq * 32 + s) * 2]);
  float L = 0.f;
  for (int s = 0; s < 32; ++s) L = fmaf(__builtin_amdgcn_exp2f(pml[(gq * 32 + s) * 2] - M), pml[(gq * 32 + s) * 2 + 1], L);
  const int d = tid & 31, sg = tid >> 5;
  float o = 0.f;
#pragma unroll
  for (int u = 0; u < 4; ++u) { const int s = sg * 4 + u; o = fmaf(__builtin_amdgcn_exp2f(pml[(gq * 32 + s) * 2] - M), po[((size_t)gq * 32 + s) * DH + sl * 32 + d], o); }
  red[sg][d] = o;
  __syncthreads();
  if (tid < 32) { float s = 0.f; for (int u = 0; u < 8; ++u) s += red[u][tid]; os[tid] = s / L; }
  __syncthreads();
  if (tid < 224) {
    float acc[8];
#pragma unroll
    for (int k = 0; k < 8; ++k) acc[k] = 0.f;
#pragma unroll
    for (int i = 0; i < 8; ++i) { const float ov = os[rg * 8 + i];
#pragma unroll
      for (int k = 0; k < 4; ++k) { acc[2 * k] = fmaf(ov, bflo(wv[i][k]), acc[2 * k]); acc[2 * k + 1] = fmaf(ov, bfhi(wv[i][k]), acc[2 * k + 1]); } }
#pragma unroll
    for (int k = 0; k < 8; ++k) yr[rg][8 * cc + k] = acc[k];
  }
  __syncthreads();
  for (int cidx = tid; cidx < D; cidx += 256) y[(size_t)b * D + cidx] = (yr[0][cidx] + yr[1][cidx]) + (yr[2][cidx] + yr[3][cidx]);
}

__global__ void __launch_bounds__(256) k_final(const float* __restrict__ y, const u16* __restrict__ x, const u16* __restrict__ gw, u16* __restrict__ out) {
  __shared__ float red[2][32][8];
  __shared__ float gsum[4];
  const int b = blockIdx.x, tid = threadIdx.x;
  float gs = 0.f;
  for (int i = tid; i < D; i += 256) gs += __builtin_bit_cast(float, (unsigned)x[i] << 16) * __builtin_bit_cast(float, (unsigned)gw[i] << 16);
  for (int o = 32; o; o >>= 1) gs += __shfl_xor(gs, o);
  if ((tid & 63) == 0) gsum[tid >> 6] = gs;
  const int cc = tid & 7, pg = tid >> 3;
  float a = 0.f, bb = 0.f;
#pragma unroll
  for (int u = 0; u < 4; ++u) { a += y[(size_t)(pg * 4 + u) * D + b * 8 + cc]; bb += y[(size_t)(128 + pg * 4 + u) * D + b * 8 + cc]; }
  red[0][pg][cc] = a; red[1][pg][cc] = bb;
  __syncthreads();
  if (tid < 8) {
    float sa = 0.f, sb = 0.f;
    for (int u = 0; u < 32; ++u) { sa += red[0][u][tid]; sb += red[1][u][tid]; }
    const float z = (gsum[0] + gsum[1]) + (gsum[2] + gsum[3]);
    const float g = 1.f / (1.f + __expf(-z));
    const float r = g * sa + (1.f - g) * sb;
    out[b * 8 + tid] = (u16)(cvtpk(r, r) & 0xffff);
  }
}

extern "C" void kernel_launch(void* const* d_in, const int* in_sizes, int n_in, void* d_out, int out_size, void* d_ws, size_t ws_size, hipStream_t stream) {
  const u16* x = (const u16*)d_in[0];
  const u16* kA = (const u16*)d_in[1]; const u16* vA = (const u16*)d_in[2]; const u16* wqA = (const u16*)d_in[3]; const u16* woA = (const u16*)d_in[4];
  const u16* kB = (const u16*)d_in[6]; const u16* vB = (const u16*)d_in[7]; const u16* wqB = (const u16*)d_in[8]; const u16* woB = (const u16*)d_in[9];
  const u16* gw = (const u16*)d_in[10];
  char* ws = (char*)d_ws;
  u16* q = (u16*)ws;
  float* pml = (float*)(ws + 16384);
  float* po = (float*)(ws + 32768);
  float* y = (float*)(ws + 32768 + (1 << 20));
  k_qproj<<<256, 256, 0, stream>>>(x, wqA, wqB, q);
  k_attn<<<256, 768, 0, stream>>>(kA, vA, kB, vB, q, po, pml);
  k_comb<<<256, 256, 0, stream>>>(po, pml, woA, woB, y);
  k_final<<<56, 256, 0, stream>>>(y, x, gw, (u16*)d_out);
}
```

```cpp
#define KV_AUX 3
#include <hip/hip_runtime.h>
#include <hip/hip_bf16.h>
#include <stdint.h>

typedef unsigned u32x4 __attribute__((ext_vector_type(4)));
typedef unsigned u32x2 __attribute__((ext_vector_type(2)));
typedef float f32x4 __attribute__((ext_vector_type(4)));
typedef float f32x2 __attribute__((ext_vector_type(2)));
typedef __bf16 bf16x2 __attribute__((ext_vector_type(2)));
typedef unsigned short u16;
typedef unsigned long long u64;
#define GAS __attribute__((address_space(1)))

constexpr int DM = 448, DH = 256, SK = 8192, NQ = 4096;
constexpr int NWG = 256, NWAVE = 12, NTHR = NWAVE * 64;
#define RING 16
#define PRE 4
constexpr int WS_G = 0;
constexpr int WS_QG = 4096;
constexpr int WS_OB1 = 131072, OB1_REC = 4096 + 128;
constexpr int WS_OB2 = WS_OB1 + NWG * OB1_REC, OB2_REC = 2048;
constexpr int WS_END = WS_OB2 + NWG * OB2_REC;
constexpr unsigned SENT = 0xFFFFFFFFu;
constexpr unsigned SPIN_CAP = 1u << 20;
constexpr int AUX_SC1 = 16;

#define STAMP(i) do { } while (0)

__device__ __forceinline__ float dot2(unsigned a, unsigned b, float acc) {
  return __builtin_amdgcn_fdot2_f32_bf16(__builtin_bit_cast(bf16x2, a), __builtin_bit_cast(bf16x2, b), acc, false);
}
__device__ __forceinline__ float bflo(unsigned u) { return __builtin_bit_cast(float, u << 16); }
__device__ __forceinline__ float bfhi(unsigned u) { return __builtin_bit_cast(float, u & 0xffff0000u); }
__device__ __forceinline__ unsigned cvtpk(float lo, float hi) {
  unsigned r; asm("v_cvt_pk_bf16_f32 %0, %1, %2" : "=v"(r) : "v"(lo), "v"(hi)); return r;
}
template <int CTRL> __device__ __forceinline__ float dpp(float x) {
  return __builtin_bit_cast(float, __builtin_amdgcn_mov_dpp(__builtin_bit_cast(int, x), CTRL, 0xf, 0xf, true));
}
constexpr int XOR1 = 0xB1, XOR2 = 0x4E, XOR7 = 0x141, XOR8 = 0x128, ROR4 = 0x124;
__device__ __forceinline__ float tr32(float a, float b) {
  auto t = __builtin_amdgcn_permlane32_swap(__float_as_uint(a), __float_as_uint(b), false, false);
  return __uint_as_float(t[0]) + __uint_as_float(t[1]);
}
__device__ __forceinline__ float tr16(float a, float b) {
  auto t = __builtin_amdgcn_permlane16_swap(__float_as_uint(a), __float_as_uint(b), false, false);
  return __uint_as_float(t[0]) + __uint_as_float(t[1]);
}
__device__ __forceinline__ float mx32(float a) {
  auto t = __builtin_amdgcn_permlane32_swap(__float_as_uint(a), __float_as_uint(a), false, false);
  return fmaxf(__uint_as_float(t[0]), __uint_as_float(t[1]));
}
__device__ __forceinline__ float mx16(float a) {
  auto t = __builtin_amdgcn_permlane16_swap(__float_as_uint(a), __float_as_uint(a), false, false);
  return fmaxf(__uint_as_float(t[0]), __uint_as_float(t[1]));
}
__device__ __forceinline__ unsigned nosent(float f) { const unsigned u = __float_as_uint(f); return u < 0xFFFFFFFEu ? u : 0xFFFFFFFEu; }
__device__ __forceinline__ u32x4 ldnt(const u32x4* p) { return __builtin_nontemporal_load(p); }
typedef __amdgpu_buffer_rsrc_t rsrc_t;
__device__ __forceinline__ u32x4 ldkv(rsrc_t r, int voff, int n) { return __builtin_amdgcn_raw_buffer_load_b128(r, voff + (n & 3) * 1024, (n >> 2) * 4096, KV_AUX); }
#define LDS_BARRIER() do { asm volatile("s_waitcnt lgkmcnt(0)" ::: "memory"); __builtin_amdgcn_s_barrier(); asm volatile("" ::: "memory"); } while (0)

struct Params {
  const u16 *x, *kA, *vA, *wqA, *woA, *kB, *vB, *wqB, *woB, *gw;
  u16* out;
  unsigned char* ws;
};

template <int NH>
__device__ __forceinline__ void wave_attn(rsrc_t kq, rsrc_t vq, u32x4 (&buf)[RING], const u32x4 (&qv)[2], int lane,
                                          float* __restrict__ sp, float* __restrict__ mrg, float* __restrict__ mlm) {
  const int c = lane & 31, hf = lane >> 5;
  float sc[NH][2];
#pragma unroll
  for (int half = 0; half < 2; ++half) {
    float xx[NH][8];
#pragma unroll
    for (int i2 = 0; i2 < 8; ++i2) {
      float dd[NH][2];
#pragma unroll
      for (int u = 0; u < 2; ++u) {
        const int n = half * 16 + i2 * 2 + u;
        const u32x4 kk = buf[n % RING];
#pragma unroll
        for (int h = 0; h < NH; ++h) { float d = dot2(kk[0], qv[h][0], 0.f); d = dot2(kk[1], qv[h][1], d); d = dot2(kk[2], qv[h][2], d); d = dot2(kk[3], qv[h][3], d); dd[h][u] = d; }
        buf[n % RING] = (n + RING < 32) ? ldkv(kq, lane * 16, n + RING) : ldkv(vq, lane * 16, n + RING - 32);
        __builtin_amdgcn_sched_barrier(0);
      }
#pragma unroll
      for (int h = 0; h < NH; ++h) xx[h][i2] = tr16(dd[h][0], dd[h][1]);
    }
#pragma unroll
    for (int h = 0; h < NH; ++h) {
#define TR_STEP(HALF, CTRL, BIT) _Pragma("unroll") for (int u = 0; u < (HALF); ++u) { \
      const float lo_ = xx[h][u], hi_ = xx[h][u + (HALF)]; const float t1_ = lo_ + dpp<CTRL>(lo_), t2_ = hi_ + dpp<CTRL>(hi_); \
      xx[h][u] = (lane & (BIT)) ? t2_ : t1_; }
      TR_STEP(4, XOR8, 8) TR_STEP(2, XOR7, 4) TR_STEP(1, XOR2, 2)
#undef TR_STEP
      const float t = xx[h][0] + dpp<XOR1>(xx[h][0]);
      sc[h][half] = t * (0.0625f * 1.4426950408889634f);
    }
  }
  const int nloc = 2 * ((lane >> 1) & 7) + ((lane >> 4) & 1) + 16 * (lane & 1);
#pragma unroll
  for (int h = 0; h < NH; ++h) {
    const float s0_ = sc[h][0], s1_ = sc[h][1];
    const float s = (lane & 1) ? s1_ : s0_;
    float m = s;
    m = fmaxf(m, dpp<XOR1>(m)); m = fmaxf(m, dpp<XOR2>(m)); m = fmaxf(m, dpp<XOR7>(m)); m = fmaxf(m, dpp<XOR8>(m)); m = mx16(m); m = mx32(m);
    const float p = __builtin_amdgcn_exp2f(s - m);
    float l = p;
    l += dpp<XOR1>(l); l += dpp<XOR2>(l); l += dpp<XOR7>(l); l += dpp<XOR8>(l); l = tr16(l, l); l = tr32(l, l);
    if (lane == 0) *(f32x2*)(mlm + h * 8) = f32x2{m, l};
    sp[h * 64 + hf * 32 + nloc] = p;
  }
  asm volatile("s_waitcnt lgkmcnt(0)" ::: "memory");
  f32x2 acc[NH][4];
#pragma unroll
  for (int h = 0; h < NH; ++h)
#pragma unroll
    for (int k = 0; k < 4; ++k) acc[h][k] = f32x2{0.f, 0.f};
#pragma unroll
  for (int i4 = 0; i4 < 8; ++i4) {
    f32x4 pr[NH];
#pragma unroll
    for (int h = 0; h < NH; ++h) pr[h] = *(const f32x4*)(sp + h * 64 + hf * 32 + 4 * i4);
#pragma unroll
    for (int u = 0; u < 4; ++u) {
      const int n = 32 + 4 * i4 + u;
      const u32x4 vv = buf[n % RING];
      f32x2 vf[4];
#pragma unroll
      for (int k = 0; k < 4; ++k) vf[k] = f32x2{bflo(vv[k]), bfhi(vv[k])};
#pragma unroll
      for (int h = 0; h < NH; ++h) {
        const f32x2 pp = f32x2{pr[h][u], pr[h][u]};
#pragma unroll
        for (int k = 0; k < 4; ++k) acc[h][k] = __builtin_elementwise_fma(pp, vf[k], acc[h][k]);
        asm volatile("" : "+v"(acc[h][0]), "+v"(acc[h][1]), "+v"(acc[h][2]), "+v"(acc[h][3]));
      }
      if (n + RING < 64) buf[n % RING] = ldkv(vq, lane * 16, n + RING - 32);
      __builtin_amdgcn_sched_barrier(0);
    }
  }
#pragma unroll
  for (int h = 0; h < NH; ++h) {
    f32x4 o;
    o[0] = tr32(acc[h][0][0], acc[h][2][0]); o[1] = tr32(acc[h][0][1], acc[h][2][1]);
    o[2] = tr32(acc[h][1][0], acc[h][3][0]); o[3] = tr32(acc[h][1][1], acc[h][3][1]);
    *(f32x4*)(mrg + h * 4 * DH + 8 * c + 4 * hf) = o;
  }
}

template <int NH>
__device__ __forceinline__ u64 stream_part(const Params& p, float* lds, rsrc_t rs, int w) {
  float* sp = lds;
  float* mrg = lds + 12 * 128;
  float* mlm = mrg + 16 * DH;
  float* qred = mlm + 32;
  const int b = blockIdx.x, j = b & 7, idx = b >> 3, myqh = idx >> 3, sl = idx & 7;
  const int tid = threadIdx.x, lane = tid & 63, ty = w >> 2, w4 = w & 3;
  const int hd = 2 * j + (myqh & 1);
  const u64 G = __hip_atomic_load((GAS u64*)(p.ws + WS_G), __ATOMIC_RELAXED, __HIP_MEMORY_SCOPE_AGENT);
  const int row0 = idx * 256 + w4 * 64;
  const u16* kb = NH == 1 ? p.kA : p.kB; const u16* vb = NH == 1 ? p.vA : p.vB;
  const int kvh = NH == 1 ? 2 * j + ty : j;
  const size_t off = ((size_t)kvh * SK + row0) * DH;
  const rsrc_t kq = __builtin_amdgcn_make_buffer_rsrc((void*)(kb + off), 0, 32768, 0x00020000);
  const rsrc_t vq = __builtin_amdgcn_make_buffer_rsrc((void*)(vb + off), 0, 32768, 0x00020000);
  u32x4 buf[RING];
  unsigned Glo, Ghi;
  if (NH == 1) {
    const bool act = w < 7;
    const int rg = (act ? tid : 0) >> 2, c4 = tid & 3;
    const u16* wq = (myqh < 2 ? p.wqA : p.wqB) + (size_t)(4 * rg) * NQ + hd * DH + sl * 32 + 8 * c4;
    const u32x2 xv = *(const u32x2*)(p.x + 4 * rg);
    u32x4 wv[4];
#pragma unroll
    for (int i = 0; i < 4; ++i) wv[i] = ldnt((const u32x4*)(wq + (size_t)i * NQ));
    __builtin_amdgcn_sched_barrier(0);
#pragma unroll
    for (int i = 0; i < PRE; ++i) buf[i] = ldkv(kq, lane * 16, i);
    __builtin_amdgcn_sched_barrier(0);
    float a[8];
#pragma unroll
    for (int k = 0; k < 4; ++k) {
      const unsigned lo01 = __builtin_amdgcn_perm(wv[1][k], wv[0][k], 0x05040100u), hi01 = __builtin_amdgcn_perm(wv[1][k], wv[0][k], 0x07060302u);
      const unsigned lo23 = __builtin_amdgcn_perm(wv[3][k], wv[2][k], 0x05040100u), hi23 = __builtin_amdgcn_perm(wv[3][k], wv[2][k], 0x07060302u);
      a[2 * k] = dot2(lo23, xv[1], dot2(lo01, xv[0], 0.f));
      a[2 * k + 1] = dot2(hi23, xv[1], dot2(hi01, xv[0], 0.f));
    }
#pragma unroll
    for (int k = 0; k < 4; ++k) a[k] = tr32(a[k], a[k + 4]);
#pragma unroll
    for (int k = 0; k < 2; ++k) a[k] = tr16(a[k], a[k + 2]);
#pragma unroll
    for (int k = 0; k < 2; ++k) { a[k] += dpp<ROR4>(a[k]); a[k] += dpp<XOR8>(a[k]); }
    if (act && (lane & 12) == 0) { const int col = 8 * c4 + 4 * (lane >> 5) + 2 * ((lane >> 4) & 1); qred[w * 32 + col] = a[0]; qred[w * 32 + col + 1] = a[1]; }
    Glo = __builtin_amdgcn_readfirstlane((unsigned)G); Ghi = __builtin_amdgcn_readfirstlane((unsigned)(G >> 32));
  } else {
    const int t = tid - 8 * 64;
    const u32x4 sv = {SENT, SENT, SENT, SENT};
    __builtin_amdgcn_raw_buffer_store_b128(sv, rs, WS_OB1 + b * OB1_REC + t * 16, 0, AUX_SC1);
    const int o2 = t < 112 ? WS_OB2 + b * OB2_REC + t * 16 : WS_OB1 + b * OB1_REC + 4096 + (t & 1) * 16;
    if (t < 114) __builtin_amdgcn_raw_buffer_store_b128(sv, rs, o2, 0, AUX_SC1);
    __builtin_amdgcn_sched_barrier(0);
#pragma unroll
    for (int i = 0; i < PRE; ++i) buf[i] = ldkv(kq, lane * 16, i);
    __builtin_amdgcn_sched_barrier(0);
    asm volatile("s_waitcnt vmcnt(%0)" :: "n"(PRE) : "memory");
    Glo = __builtin_amdgcn_readfirstlane((unsigned)G); Ghi = __builtin_amdgcn_readfirstlane((unsigned)(G >> 32));
  }
  LDS_BARRIER();
  const u64 T = (((u64)Ghi << 32) | Glo) + 1;
  const u64 tag = T << 16;
  if (NH == 1 && w == 0) {
    STAMP(1);
    if (lane < 32) {
      float s = 0.f;
#pragma unroll
      for (int u = 0; u < 7; ++u) s += qred[u * 32 + lane];
      const u64 gr = tag | (u64)(cvtpk(s, s) & 0xffffu);
      const int gcol = (myqh < 2 ? 0 : NQ) + hd * DH + sl * 32 + lane;
      __hip_atomic_store((GAS u64*)(p.ws + WS_QG) + gcol, gr, __ATOMIC_RELAXED, __HIP_MEMORY_SCOPE_AGENT);
    }
  }
  __builtin_amdgcn_sched_barrier(0);
#pragma unroll
  for (int i = PRE; i < RING; ++i) buf[i] = ldkv(kq, lane * 16, i);
  __builtin_amdgcn_sched_barrier(0);
  u32x4 qv[2];
  {
    constexpr int nh = NH;
    const int gcol = (NH == 1 ? (2 * j + ty) * DH : NQ + 2 * j * DH) + 8 * (lane & 31);
    const unsigned tlo = (unsigned)tag, thi = (unsigned)(tag >> 32);
    for (unsigned spins = 0; spins < SPIN_CAP; ++spins) {
      asm volatile("" ::: "memory");
      u32x4 g[4];
#pragma unroll
      for (int u = 0; u < 4; ++u) g[u] = __builtin_amdgcn_raw_buffer_load_b128(rs, WS_QG + gcol * 8 + 16 * u, 0, AUX_SC1);
      unsigned bad = 0;
#pragma unroll
      for (int u = 0; u < 4; ++u) {
        bad |= (g[u][1] ^ thi) | (g[u][3] ^ thi) | (((g[u][0] ^ tlo) | (g[u][2] ^ tlo)) >> 16);
        qv[0][u] = __builtin_amdgcn_perm(g[u][2], g[u][0], 0x05040100u);
      }
      if (nh == 2) {
#pragma unroll
        for (int u = 0; u < 4; ++u) g[u] = __builtin_amdgcn_raw_buffer_load_b128(rs, WS_QG + (gcol + DH) * 8 + 16 * u, 0, AUX_SC1);
#pragma unroll
        for (int u = 0; u < 4; ++u) {
          bad |= (g[u][1] ^ thi) | (g[u][3] ^ thi) | (((g[u][0] ^ tlo) | (g[u][2] ^ tlo)) >> 16);
          qv[1][u] = __builtin_amdgcn_perm(g[u][2], g[u][0], 0x05040100u);
        }
      } else qv[1] = qv[0];
      if (__builtin_amdgcn_ballot_w64(bad != 0) == 0) break;
      __builtin_amdgcn_s_sleep(2);
    }
  }
  if (w == 0) STAMP(2);
  if (w == 8) STAMP(3);
  wave_attn<NH>(kq, vq, buf, qv, lane, sp + w * 128, mrg + (ty * 4 + w4) * DH, mlm + (ty * 4 + w4) * 2);
  return T;
}

__global__ void __launch_bounds__(NTHR, 3) k_fused(Params p) {
  __shared__ __attribute__((aligned(16))) float lds[12 * 128 + 16 * DH + 32 + 7 * 32];
  float* mrg = lds + 12 * 128;
  float* mlm = mrg + 16 * DH;
  const int b = blockIdx.x, j = b & 7, idx = b >> 3, myqh = idx >> 3, sl = idx & 7;
  const int tid = threadIdx.x, lane = tid & 63, w = __builtin_amdgcn_readfirstlane(tid >> 6);
  const __amdgpu_buffer_rsrc_t rs = __builtin_amdgcn_make_buffer_rsrc((void*)p.ws, 0, WS_END, 0x00020000);
  const int hd = 2 * j + (myqh & 1);
  if (w >= 8) { stream_part<2>(p, lds, rs, w); LDS_BARRIER(); return; }
  const u64 T = stream_part<1>(p, lds, rs, w);
  const u64 tag = T << 16;
  if (w == 0) STAMP(4);
  if (w == 4) STAMP(5);
  if (w >= 4) { LDS_BARRIER(); return; }
  unsigned wo[32];
  const int cp = tid;
  {
    const u16* wop = (myqh < 2 ? p.woA : p.woB) + (size_t)(hd * DH + sl * 32) * DM + 2 * (cp < 224 ? cp : 0);
#pragma unroll
    for (int i = 0; i < 32; ++i) wo[i] = __builtin_nontemporal_load((const unsigned*)(wop + (size_t)i * DM));
  }
  float gate = 0.f;
  if (w == 0 && b < 56) {
    const unsigned tlo = (unsigned)tag, thi = (unsigned)(tag >> 32);
    for (unsigned spins = 0; spins < SPIN_CAP; ++spins) {
      asm volatile("" ::: "memory");
      unsigned bad = 0;
#pragma unroll
      for (int u = 0; u < 4; ++u) { const u32x2 g = __builtin_amdgcn_raw_buffer_load_b64(rs, WS_QG + (lane + 64 * u) * 256, 0, AUX_SC1); bad |= (g[1] ^ thi) | ((g[0] ^ tlo) >> 16); }
      if (__builtin_amdgcn_ballot_w64(bad != 0) == 0) break;
      __builtin_amdgcn_s_sleep(2);
    }
    float z = 0.f;
    if (lane < 56) {
      const u32x4 xa = *(const u32x4*)(p.x + 8 * lane), ga = *(const u32x4*)(p.gw + 8 * lane);
      z = dot2(xa[0], ga[0], z); z = dot2(xa[1], ga[1], z); z = dot2(xa[2], ga[2], z); z = dot2(xa[3], ga[3], z);
    }
    z += dpp<XOR1>(z); z += dpp<XOR2>(z); z += dpp<XOR7>(z); z += dpp<XOR8>(z); z = tr16(z, z); z = tr32(z, z);
    gate = 1.f / (1.f + __expf(-z));
  }
  unsigned wl[16], wh[16];
#pragma unroll
  for (int i = 0; i < 16; ++i) { wl[i] = __builtin_amdgcn_perm(wo[2 * i + 1], wo[2 * i], 0x05040100u); wh[i] = __builtin_amdgcn_perm(wo[2 * i + 1], wo[2 * i], 0x07060302u); }
  LDS_BARRIER();
  if (w == 0) STAMP(6);
  {
    const float m0 = mlm[w * 8], m1 = mlm[w * 8 + 2], m2 = mlm[w * 8 + 4], m3 = mlm[w * 8 + 6];
    const float M = fmaxf(fmaxf(m0, m1), fmaxf(m2, m3));
    const float e0 = __builtin_amdgcn_exp2f(m0 - M), e1 = __builtin_amdgcn_exp2f(m1 - M), e2 = __builtin_amdgcn_exp2f(m2 - M), e3 = __builtin_amdgcn_exp2f(m3 - M);
    const float L = e0 * mlm[w * 8 + 1] + e1 * mlm[w * 8 + 3] + e2 * mlm[w * 8 + 5] + e3 * mlm[w * 8 + 7];
    const float* mp = mrg + w * 4 * DH + 4 * lane;
    const f32x4 o = e0 * *(const f32x4*)mp + e1 * *(const f32x4*)(mp + DH) + e2 * *(const f32x4*)(mp + 2 * DH) + e3 * *(const f32x4*)(mp + 3 * DH);
    const u32x4 ov = {nosent(o[0]), nosent(o[1]), nosent(o[2]), nosent(o[3])};
    __builtin_amdgcn_raw_buffer_store_b128(ov, rs, WS_OB1 + b * OB1_REC + w * 1024 + lane * 16, 0, AUX_SC1);
    if (lane == 0) { const u32x2 mlv = {nosent(M), nosent(L)}; __builtin_amdgcn_raw_buffer_store_b64(mlv, rs, WS_OB1 + b * OB1_REC + 4096 + w * 8, 0, AUX_SC1); }
  }
  float y0, y1;
  {
    u32x4 ov[4]; u32x2 ml[4];
    for (unsigned spins = 0; spins < SPIN_CAP; ++spins) {
      asm volatile("" ::: "memory");
#pragma unroll
      for (int i = 0; i < 4; ++i) {
        const int pb = (((lane >> 3) + 8 * i) << 3) + j;
        ov[i] = __builtin_amdgcn_raw_buffer_load_b128(rs, WS_OB1 + pb * OB1_REC + myqh * 1024 + sl * 128 + (lane & 7) * 16, 0, AUX_SC1);
        ml[i] = __builtin_amdgcn_raw_buffer_load_b64(rs, WS_OB1 + pb * OB1_REC + 4096 + myqh * 8, 0, AUX_SC1);
      }
      unsigned mxv = 0;
#pragma unroll
      for (int i = 0; i < 4; ++i) mxv = max(max(max(mxv, ov[i][0]), max(ov[i][1], ov[i][2])), max(ov[i][3], max(ml[i][0], ml[i][1])));
      if (__builtin_amdgcn_ballot_w64(mxv == SENT) == 0) break;
    }
    if (w == 0) STAMP(7);
    float M = fmaxf(fmaxf(__uint_as_float(ml[0][0]), __uint_as_float(ml[1][0])), fmaxf(__uint_as_float(ml[2][0]), __uint_as_float(ml[3][0])));
    M = fmaxf(M, dpp<XOR8>(M)); M = mx16(M); M = mx32(M);
    float L = 0.f; float o[4] = {0.f, 0.f, 0.f, 0.f};
#pragma unroll
    for (int i = 0; i < 4; ++i) {
      const float e = __builtin_amdgcn_exp2f(__uint_as_float(ml[i][0]) - M);
      L = fmaf(e, __uint_as_float(ml[i][1]), L);
#pragma unroll
      for (int k = 0; k < 4; ++k) o[k] = fmaf(e, __uint_as_float(ov[i][k]), o[k]);
    }
    L += dpp<XOR8>(L); L = tr16(L, L); L = tr32(L, L);
    const float inv = 1.f / L;
    float r0 = tr32(o[0], o[2]), r1 = tr32(o[1], o[3]);
    r0 = tr16(r0, r0); r1 = tr16(r1, r1);
    r0 += dpp<XOR8>(r0); r1 += dpp<XOR8>(r1);
    const unsigned pk = cvtpk(r0 * inv, r1 * inv);
    y0 = 0.f; y1 = 0.f;
#pragma unroll
    for (int i = 0; i < 16; ++i) {
      const unsigned s = __builtin_amdgcn_readlane(pk, (i >> 1) + 32 * (i & 1));
      y0 = dot2(wl[i], s, y0); y1 = dot2(wh[i], s, y1);
    }
  }
  if (cp < 224) { const u32x2 yv = {nosent(y0), nosent(y1)}; __builtin_amdgcn_raw_buffer_store_b64(yv, rs, WS_OB2 + b * OB2_REC + cp * 8, 0, AUX_SC1); }
  if (w == 0) STAMP(8);
  if (w == 0 && b < 56) {
    u32x4 yv[8];
    for (unsigned spins = 0; spins < SPIN_CAP; ++spins) {
      asm volatile("" ::: "memory");
#pragma unroll
      for (int i = 0; i < 8; ++i) yv[i] = __builtin_amdgcn_raw_buffer_load_b128(rs, WS_OB2 + ((lane >> 1) + 32 * i) * OB2_REC + b * 32 + (lane & 1) * 16, 0, AUX_SC1);
      unsigned mxv = 0;
#pragma unroll
      for (int i = 0; i < 8; ++i) mxv = max(max(max(mxv, yv[i][0]), max(yv[i][1], yv[i][2])), yv[i][3]);
      if (__builtin_amdgcn_ballot_w64(mxv == SENT) == 0) break;
    }
    STAMP(9);
    float r[4];
#pragma unroll
    for (int k = 0; k < 4; ++k) {
      const float a = (__uint_as_float(yv[0][k]) + __uint_as_float(yv[1][k])) + (__uint_as_float(yv[2][k]) + __uint_as_float(yv[3][k]));
      const float bb = (__uint_as_float(yv[4][k]) + __uint_as_float(yv[5][k])) + (__uint_as_float(yv[6][k]) + __uint_as_float(yv[7][k]));
      r[k] = gate * a + (1.f - gate) * bb;
    }
    float r0 = tr32(r[0], r[2]), r1 = tr32(r[1], r[3]);
    r0 = tr16(r0, r0); r1 = tr16(r1, r1);
    r0 += dpp<XOR8>(r0); r1 += dpp<XOR8>(r1);
    r0 += dpp<ROR4>(r0); r1 += dpp<ROR4>(r1);
    r0 += dpp<XOR2>(r0); r1 += dpp<XOR2>(r1);
    if ((lane & 30) == 0) *(unsigned*)(p.out + 8 * b + 4 * (lane & 1) + 2 * (lane >> 5)) = cvtpk(r0, r1);
    if (b == 0 && lane == 0) __hip_atomic_store((GAS u64*)(p.ws + WS_G), T, __ATOMIC_RELAXED, __HIP_MEMORY_SCOPE_AGENT);
    STAMP(10);
  }
}

extern "C" void kernel_launch(void* const* d_in, const int* in_sizes, int n_in, void* d_out, int out_size, void* d_ws, size_t ws_size, hipStream_t stream) {
  Params p;
  p.x = (const u16*)d_in[0];
  p.kA = (const u16*)d_in[1]; p.vA = (const u16*)d_in[2]; p.wqA = (const u16*)d_in[3]; p.woA = (const u16*)d_in[4];
  p.kB = (const u16*)d_in[6]; p.vB = (const u16*)d_in[7]; p.wqB = (const u16*)d_in[8]; p.woB = (const u16*)d_in[9];
  p.gw = (const u16*)d_in[10];
  p.out = (u16*)d_out; p.ws = (unsigned char*)d_ws;
  k_fused<<<NWG, NTHR, 0, stream>>>(p);
}
```

```cpp
#include <hip/hip_runtime.h>
#include <hip/hip_bf16.h>
#include <stdint.h>

typedef unsigned u32x4 __attribute__((ext_vector_type(4)));
typedef unsigned u32x2 __attribute__((ext_vector_type(2)));
typedef float f32x4 __attribute__((ext_vector_type(4)));
typedef float f32x2 __attribute__((ext_vector_type(2)));
typedef __bf16 bf16x2 __attribute__((ext_vector_type(2)));
typedef unsigned short u16;
typedef unsigned long long u64;
typedef __amdgpu_buffer_rsrc_t rsrc_t;
#define GAS __attribute__((address_space(1)))
#define LAS __attribute__((address_space(3)))

constexpr int DM = 448, DH = 256, SK = 8192, NQ = 4096;
constexpr int NWG = 256, NTHR = 512;
#define PACE 7
#define WOSLEEP 64
#define WQMODE 0
#define SLOWSLEEP 8
#define FASTGAP 6
#define GATE 8
constexpr int WS_G = 0;
constexpr int WS_QG = 4096;
constexpr int WS_OB1 = WS_QG + 32768;
constexpr int WS_OB2 = WS_OB1 + 32 * 12 * 1024, OB2_REC = 1792;
constexpr int WS_END = WS_OB2 + NWG * OB2_REC;
constexpr int NFIN = 56;
constexpr unsigned SPIN_CAP = 1u << 17;
constexpr int AUX_SC1 = 16, AUX_NT = 2;
constexpr int L_MRG = 0;
constexpr int L_ML = 16 * DH;
constexpr int L_QRED = L_ML + 32;
constexpr int L_Q = L_QRED + 128;
constexpr int L_CNT = L_Q + 512;
constexpr int L_O = L_CNT + 4;
constexpr int L_FIN = L_O + 16;
constexpr int L_WO = L_FIN + 32;
constexpr int L_END = L_WO + 4 * 2048;

#define STAMP(i) do { } while (0)
#define STAMP_FLUSH(lo, hi) do { } while (0)

__device__ __forceinline__ float dot2(unsigned a, unsigned b, float acc) {
  return __builtin_amdgcn_fdot2_f32_bf16(__builtin_bit_cast(bf16x2, a), __builtin_bit_cast(bf16x2, b), acc, false);
}
__device__ __forceinline__ float bflo(unsigned u) { return __builtin_bit_cast(float, u << 16); }
__device__ __forceinline__ float bfhi(unsigned u) { return __builtin_bit_cast(float, u & 0xffff0000u); }
__device__ __forceinline__ unsigned cvtpk(float lo, float hi) {
  unsigned r; asm("v_cvt_pk_bf16_f32 %0, %1, %2" : "=v"(r) : "v"(lo), "v"(hi)); return r;
}
template <int CTRL> __device__ __forceinline__ float dpp(float x) {
  return __builtin_bit_cast(float, __builtin_amdgcn_mov_dpp(__builtin_bit_cast(int, x), CTRL, 0xf, 0xf, true));
}
constexpr int XOR1 = 0xB1, XOR2 = 0x4E, XOR7 = 0x141, XOR8 = 0x128, ROR4 = 0x124;
__device__ __forceinline__ float tr32(float a, float b) {
  auto t = __builtin_amdgcn_permlane32_swap(__float_as_uint(a), __float_as_uint(b), false, false);
  return __uint_as_float(t[0]) + __uint_as_float(t[1]);
}
__device__ __forceinline__ float tr16(float a, float b) {
  auto t = __builtin_amdgcn_permlane16_swap(__float_as_uint(a), __float_as_uint(b), false, false);
  return __uint_as_float(t[0]) + __uint_as_float(t[1]);
}
__device__ __forceinline__ float mx32(float a) {
  auto t = __builtin_amdgcn_permlane32_swap(__float_as_uint(a), __float_as_uint(a), false, false);
  return fmaxf(__uint_as_float(t[0]), __uint_as_float(t[1]));
}
__device__ __forceinline__ float mx16(float a) {
  auto t = __builtin_amdgcn_permlane16_swap(__float_as_uint(a), __float_as_uint(a), false, false);
  return fmaxf(__uint_as_float(t[0]), __uint_as_float(t[1]));
}
__device__ __forceinline__ float rowsum16(float x) { x += dpp<XOR1>(x); x += dpp<XOR2>(x); x += dpp<XOR7>(x); x += dpp<XOR8>(x); return x; }
#define SCHED() __builtin_amdgcn_sched_barrier(0)
#define BARRIER() do { SCHED(); asm volatile("s_waitcnt lgkmcnt(0)" ::: "memory"); __builtin_amdgcn_s_barrier(); asm volatile("" ::: "memory"); SCHED(); } while (0)
__device__ __forceinline__ u32x4 sel4(bool c, u32x4 a, u32x4 b) { return u32x4{c ? a[0] : b[0], c ? a[1] : b[1], c ? a[2] : b[2], c ? a[3] : b[3]}; }
#define VMWAIT(n) asm volatile("s_waitcnt vmcnt(%0)" :: "n"(n) : "memory")

struct PollRegs { u32x4 r[6]; };
template <int SH>
__device__ __forceinline__ void gather2(rsrc_t rs, u32x4 rsw, int o0, int o1, bool m0, bool m1, unsigned tagw, int few, u32x4 (&g)[2], PollRegs& P) {
  g[0] = u32x4{0u, 0u, 0u, 0u}; g[1] = g[0];
  unsigned spins = 0;
  int left;
#pragma nounroll
  for (;;) {
    asm volatile("" ::: "memory");
    u32x4 a = __builtin_amdgcn_raw_buffer_load_b128(rs, m0 ? o0 : WS_END, 0, AUX_SC1), c = __builtin_amdgcn_raw_buffer_load_b128(rs, m1 ? o1 : WS_END, 0, AUX_SC1);
    asm volatile("" : "+v"(a), "+v"(c));
    g[0] = sel4(m0, a, g[0]); g[1] = sel4(m1, c, g[1]);
    m0 = m0 & ((((a[1] ^ tagw) | (a[3] ^ tagw)) >> SH) != 0); m1 = m1 & ((((c[1] ^ tagw) | (c[3] ^ tagw)) >> SH) != 0);
    left = __builtin_popcountll(__builtin_amdgcn_ballot_w64(m0)) + __builtin_popcountll(__builtin_amdgcn_ballot_w64(m1));
    if (left <= few || ++spins >= SPIN_CAP) break;
    __builtin_amdgcn_s_sleep(SLOWSLEEP);
  }
  u32x4 h0, h1;
  {
    const int a0 = m0 ? o0 : WS_END, a1 = m1 ? o1 : WS_END;
    const unsigned e0 = m0 ? tagw : 0u, e1 = m1 ? tagw : 0u;
    unsigned t0, t1, cnt = left ? SPIN_CAP : 0u;
#define PL(A, B) "buffer_load_dwordx4 v[" #A ":" #A "+3], %[a0], %[rs], 0 offen sc1\n\tbuffer_load_dwordx4 v[" #B ":" #B "+3], %[a1], %[rs], 0 offen sc1\n\t"
#define PC(A, B, L) "s_waitcnt vmcnt(4)\n\tv_bitop3_b32 %[t0], v[" #A "+1], v[" #A "+3], %[e0] bitop3:0x7e\n\tv_bitop3_b32 %[t1], v[" #B "+1], v[" #B "+3], %[e1] bitop3:0x7e\n\t" \
                    "v_or_b32 %[t0], %[t0], %[t1]\n\tv_cmp_lt_u32 vcc, %[lim], %[t0]\n\ts_cbranch_vccz " L "%=\n\t"
#define PM(A, B) "v_mov_b32 v128, v[" #A "]\n\tv_mov_b32 v129, v[" #A "+1]\n\tv_mov_b32 v130, v[" #A "+2]\n\tv_mov_b32 v131, v[" #A "+3]\n\t" \
                 "v_mov_b32 v132, v[" #B "]\n\tv_mov_b32 v133, v[" #B "+1]\n\tv_mov_b32 v134, v[" #B "+2]\n\tv_mov_b32 v135, v[" #B "+3]\n\t"
    asm volatile(
        "s_cmp_eq_u32 %[cnt], 0\n\ts_cbranch_scc1 .Lpe%=\n\t"
        "s_nop 4\n\t"
        PL(136, 140) "s_sleep %[gap]\n\t" PL(144, 148) "s_sleep %[gap]\n\t" PL(152, 156)
        ".Lpl%=:\n\t"
        PC(136, 140, ".Lpa") PL(136, 140)
        PC(144, 148, ".Lpb") PL(144, 148)
        PC(152, 156, ".Lpc") PL(152, 156)
        "s_sub_u32 %[cnt], %[cnt], 1\n\ts_cmp_lg_u32 %[cnt], 0\n\ts_cbranch_scc1 .Lpl%=\n\t"
        "s_waitcnt vmcnt(0)\n\t"
        ".Lpa%=:\n\t" PM(136, 140) "s_branch .Lpe%=\n\t"
        ".Lpb%=:\n\t" PM(144, 148) "s_branch .Lpe%=\n\t"
        ".Lpc%=:\n\t" PM(152, 156)
        ".Lpe%=:\n\t"
        : "=&{v[128:131]}"(h0), "=&{v[132:135]}"(h1), "=&{v[136:139]}"(P.r[0]), "=&{v[140:143]}"(P.r[1]), "=&{v[144:147]}"(P.r[2]), "=&{v[148:151]}"(P.r[3]),
          "=&{v[152:155]}"(P.r[4]), "=&{v[156:159]}"(P.r[5]), [t0] "=&v"(t0), [t1] "=&v"(t1), [cnt] "+s"(cnt)
        : [a0] "v"(a0), [a1] "v"(a1), [e0] "v"(e0), [e1] "v"(e1), [rs] "s"(rsw), [lim] "n"((1 << SH) - 1), [gap] "n"(FASTGAP)
        : "vcc", "scc", "memory");
#undef PL
#undef PC
#undef PM
  }
  g[0] = sel4(m0, h0, g[0]); g[1] = sel4(m1, h1, g[1]);
}
#define KEEP(P) asm volatile("" :: "{v[136:139]}"(P.r[0]), "{v[140:143]}"(P.r[1]), "{v[144:147]}"(P.r[2]), "{v[148:151]}"(P.r[3]), "{v[152:155]}"(P.r[4]), "{v[156:159]}"(P.r[5]))

struct Params {
  const u16 *x, *kA, *vA, *wqA, *woA, *kB, *vB, *wqB, *woB, *gw;
  u16* out;
  unsigned char* ws;
};

struct St { float m[2], l[2]; f32x2 acc[2][4]; };

template <int NH, int MODE>
__device__ __forceinline__ void do_chunk(u32x4 (&d)[8], const u32x4 (&qv)[2], St& s, rsrc_t kr, rsrc_t vr, int voff, int so) {
  constexpr float C2 = 0.0625f * 1.4426950408889634f;
  float sc[NH][4];
#pragma unroll
  for (int i = 0; i < 4; ++i) {
#pragma unroll
    for (int h = 0; h < NH; ++h) { float t = dot2(d[i][0], qv[h][0], 0.f); t = dot2(d[i][1], qv[h][1], t); t = dot2(d[i][2], qv[h][2], t); sc[h][i] = dot2(d[i][3], qv[h][3], t); }
    if (MODE) {
#pragma unroll
      for (int h = 0; h < NH; ++h) asm volatile("" : "+v"(sc[h][i]));
      if (MODE == 2) VMWAIT(GATE - 1);
      d[i] = __builtin_amdgcn_raw_buffer_load_b128(kr, voff + i * 1024, so, AUX_NT);
      SCHED();
    }
  }
  float pr[NH][4];
#pragma unroll
  for (int h = 0; h < NH; ++h) {
    float a = rowsum16(tr16(sc[h][0], sc[h][1])) * C2, b = rowsum16(tr16(sc[h][2], sc[h][3])) * C2;
    const float mn = fmaxf(mx16(fmaxf(a, b)), s.m[h]);
    const float al = __builtin_amdgcn_exp2f(s.m[h] - mn);
    a = __builtin_amdgcn_exp2f(a - mn); b = __builtin_amdgcn_exp2f(b - mn);
    s.l[h] = fmaf(s.l[h], al, a + b);
    s.m[h] = mn;
    auto ta = __builtin_amdgcn_permlane16_swap(__float_as_uint(a), __float_as_uint(a), false, false);
    auto tb = __builtin_amdgcn_permlane16_swap(__float_as_uint(b), __float_as_uint(b), false, false);
    pr[h][0] = __uint_as_float(ta[0]); pr[h][1] = __uint_as_float(ta[1]); pr[h][2] = __uint_as_float(tb[0]); pr[h][3] = __uint_as_float(tb[1]);
    const f32x2 al2 = {al, al};
#pragma unroll
    for (int k = 0; k < 4; ++k) s.acc[h][k] *= al2;
  }
#pragma unroll
  for (int i = 0; i < 4; ++i) {
    f32x2 vf[4];
#pragma unroll
    for (int k = 0; k < 4; ++k) vf[k] = f32x2{bflo(d[4 + i][k]), bfhi(d[4 + i][k])};
#pragma unroll
    for (int h = 0; h < NH; ++h) {
      const f32x2 pp = f32x2{pr[h][i], pr[h][i]};
#pragma unroll
      for (int k = 0; k < 4; ++k) s.acc[h][k] = __builtin_elementwise_fma(pp, vf[k], s.acc[h][k]);
      asm volatile("" : "+v"(s.acc[h][0]), "+v"(s.acc[h][1]), "+v"(s.acc[h][2]), "+v"(s.acc[h][3]));
    }
    if (MODE) {
      if (MODE == 2) VMWAIT(GATE - 1);
      d[4 + i] = __builtin_amdgcn_raw_buffer_load_b128(vr, voff + i * 1024, so, AUX_NT);
    }
    SCHED();
  }
}

template <int NH>
__device__ __forceinline__ void flush_seg(float* lds, int slot, int lane, St& s) {
#pragma unroll
  for (int h = 0; h < NH; ++h) {
    const float M = mx32(s.m[h]);
    const float e = __builtin_amdgcn_exp2f(s.m[h] - M);
    float L = s.l[h] * e; L = tr16(L, L); L = tr32(L, L);
    const f32x2 e2 = {e, e};
    const f32x2 a0 = s.acc[h][0] * e2, a1 = s.acc[h][1] * e2, a2 = s.acc[h][2] * e2, a3 = s.acc[h][3] * e2;
    f32x4 o;
    o[0] = tr32(a0[0], a2[0]); o[1] = tr32(a0[1], a2[1]); o[2] = tr32(a1[0], a3[0]); o[3] = tr32(a1[1], a3[1]);
    *(f32x4*)(lds + L_MRG + (slot + 4 * h) * DH + 8 * (lane & 31) + 4 * (lane >> 5)) = o;
    *(f32x2*)(lds + L_ML + (slot + 4 * h) * 2) = f32x2{M, L};
    s.m[h] = -INFINITY; s.l[h] = 0.f;
#pragma unroll
    for (int k = 0; k < 4; ++k) s.acc[h][k] = f32x2{0.f, 0.f};
  }
}

__device__ __forceinline__ int gchunk(int idx, int w4, int n) { return idx * 96 + (n >> 3) * 32 + (((n + idx) & 7) << 2) + w4; }

template <bool FIRSTB>
__device__ __forceinline__ void stream(const Params& p, float* lds, int j, int idx, int w4, int lane
) {
  const int voff = lane * 16;
  const rsrc_t kB = __builtin_amdgcn_make_buffer_rsrc((void*)(p.kB + (size_t)j * SK * DH), 0, SK * DH * 2, 0x00020000);
  const rsrc_t vB = __builtin_amdgcn_make_buffer_rsrc((void*)(p.vB + (size_t)j * SK * DH), 0, SK * DH * 2, 0x00020000);
  const rsrc_t kA = __builtin_amdgcn_make_buffer_rsrc((void*)(p.kA + (size_t)2 * j * SK * DH), 0, 2 * SK * DH * 2, 0x00020000);
  const rsrc_t vA = __builtin_amdgcn_make_buffer_rsrc((void*)(p.vA + (size_t)2 * j * SK * DH), 0, 2 * SK * DH * 2, 0x00020000);
  const rsrc_t k0 = FIRSTB ? kB : kA, v0 = FIRSTB ? vB : vA;
  constexpr int C0 = FIRSTB ? 0 : 1024;
  constexpr int NH0 = FIRSTB ? 2 : 1;
  u32x4 buf[4][8];
  BARRIER();
#pragma unroll
  for (int n = 0; n < 32; ++n) {
    if (n >= PACE) VMWAIT(PACE - 1);
    buf[n >> 3][n & 7] = __builtin_amdgcn_raw_buffer_load_b128((n & 7) < 4 ? k0 : v0, voff + (n & 3) * 1024, (gchunk(idx, w4, n >> 3) - C0) * 4096, AUX_NT);
    SCHED();
  }
  STAMP(1);
  BARRIER();
  STAMP(2);
  u32x4 qv[2];
  St s;
#pragma unroll
  for (int h = 0; h < 2; ++h) {
    s.m[h] = -INFINITY; s.l[h] = 0.f;
#pragma unroll
    for (int k = 0; k < 4; ++k) s.acc[h][k] = f32x2{0.f, 0.f};
  }
  int qh = FIRSTB ? 2 : (idx <= 21 ? 0 : 1);
  qv[0] = *(const u32x4*)(lds + L_Q + qh * 128 + 4 * (lane & 31));
  qv[1] = *(const u32x4*)(lds + L_Q + 3 * 128 + 4 * (lane & 31));
  do_chunk<NH0, 1>(buf[0], qv, s, k0, v0, voff, (gchunk(idx, w4, 4) - C0) * 4096);
  STAMP(3);
  do_chunk<NH0, 0>(buf[1], qv, s, k0, v0, voff, 0);
  STAMP(4);
  do_chunk<NH0, 2>(buf[2], qv, s, k0, v0, voff, (gchunk(idx, w4, 5) - C0) * 4096);
  do_chunk<NH0, 0>(buf[3], qv, s, k0, v0, voff, 0);
  int n = 4;
  if (FIRSTB) {
    const int nb = idx < 10 ? 22 : 16;
#pragma nounroll
    for (; n < nb; n += 2) {
      const int c = gchunk(idx, w4, n + 2), c2 = gchunk(idx, w4, n + 3);
      const bool isb = c < 1024;
      const rsrc_t kr = isb ? kB : kA, vr = isb ? vB : vA;
      do_chunk<2, 2>(buf[0], qv, s, kr, vr, voff, (c & 1023) * 4096);
      do_chunk<2, 2>(buf[2], qv, s, kr, vr, voff, (c2 & 1023) * 4096);
    }
    if (idx < 10) {
      do_chunk<2, 0>(buf[0], qv, s, kB, vB, voff, 0);
      do_chunk<2, 0>(buf[2], qv, s, kB, vB, voff, 0);
      flush_seg<2>(lds, 8 + w4, lane, s);
      return;
    }
    flush_seg<2>(lds, 8 + w4, lane, s);
    qh = 0;
    qv[0] = *(const u32x4*)(lds + L_Q + 4 * (lane & 31));
  }
#pragma nounroll
  for (; n < 22; n += 2) {
    if (!FIRSTB && idx == 21 && n == 8) {
      flush_seg<1>(lds, w4, lane, s);
      qh = 1;
      qv[0] = *(const u32x4*)(lds + L_Q + 128 + 4 * (lane & 31));
    }
    do_chunk<1, 2>(buf[0], qv, s, kA, vA, voff, (gchunk(idx, w4, n + 2) - 1024) * 4096);
    do_chunk<1, 2>(buf[2], qv, s, kA, vA, voff, (gchunk(idx, w4, n + 3) - 1024) * 4096);
  }
  do_chunk<1, 0>(buf[0], qv, s, kA, vA, voff, 0);
  do_chunk<1, 0>(buf[2], qv, s, kA, vA, voff, 0);
  flush_seg<1>(lds, qh * 4 + w4, lane, s);
}

__global__ void __launch_bounds__(NTHR, 2) k_fused(Params p) {
  __shared__ __attribute__((aligned(16))) float lds[L_END];
  const int b = blockIdx.x, j = b & 7, idx = b >> 3;
  const int tid = threadIdx.x, lane = tid & 63, w = __builtin_amdgcn_readfirstlane(tid >> 6);
#define TSTARG
  asm volatile("" :: "s"(p.x), "s"(p.kA), "s"(p.vA), "s"(p.wqA), "s"(p.woA), "s"(p.kB), "s"(p.vB), "s"(p.wqB), "s"(p.woB), "s"(p.gw), "s"(p.out), "s"(p.ws));
  if (w >= 4) {
    if (idx <= 10) { stream<true>(p, lds, j, idx, w - 4, lane TSTARG); STAMP(12); BARRIER(); if (w == 4) { STAMP_FLUSH(1, 5); STAMP_FLUSH(12, 13); } return; }
    stream<false>(p, lds, j, idx, w - 4, lane TSTARG); STAMP(12); BARRIER();
    if (w == 4) { STAMP_FLUSH(1, 5); STAMP_FLUSH(12, 13); }
    return;
  }
  const rsrc_t rs = __builtin_amdgcn_make_buffer_rsrc((void*)p.ws, 0, WS_END, 0x00020000);
  const int myqh = idx >> 3, sl = idx & 7, hd = 2 * j + (myqh & 1);
  const int pfirst = w == 0 ? 10 : (w == 1 ? 21 : 0), plast = w == 0 ? 21 : (w == 1 ? 31 : 10);
  const bool mine = idx >= pfirst && idx <= plast;
  unsigned tag;
  {
    const bool act = tid < 224;
    const int rg = (act ? tid : 0) >> 2, c4 = tid & 3;
    const u16* wq = (myqh < 2 ? p.wqA : p.wqB) + (size_t)(8 * rg) * NQ + hd * DH + sl * 32 + 8 * c4;
    u32x4 wv[8];
#pragma unroll
    for (int i = 0; i < 8; ++i) wv[i] = __builtin_nontemporal_load((const u32x4*)(wq + (size_t)i * NQ));
    u32x4 xv = *(const u32x4*)(p.x + 8 * rg);
    const unsigned G = __hip_atomic_load((GAS unsigned*)(p.ws + WS_G), __ATOMIC_RELAXED, __HIP_MEMORY_SCOPE_AGENT);
    if (tid < 4) *(LAS unsigned*)(lds + L_CNT + tid) = 0u;
    BARRIER();
    if (!act) xv = u32x4{0u, 0u, 0u, 0u};
    float a[8];
#pragma unroll
    for (int k = 0; k < 4; ++k) {
      float lo = 0.f, hi = 0.f;
#pragma unroll
      for (int i = 0; i < 4; ++i) {
        lo = dot2(__builtin_amdgcn_perm(wv[2 * i + 1][k], wv[2 * i][k], 0x05040100u), xv[i], lo);
        hi = dot2(__builtin_amdgcn_perm(wv[2 * i + 1][k], wv[2 * i][k], 0x07060302u), xv[i], hi);
      }
      a[2 * k] = lo; a[2 * k + 1] = hi;
    }
#pragma unroll
    for (int k = 0; k < 4; ++k) a[k] = tr32(a[k], a[k + 4]);
#pragma unroll
    for (int k = 0; k < 2; ++k) a[k] = tr16(a[k], a[k + 2]);
#pragma unroll
    for (int k = 0; k < 2; ++k) { a[k] += dpp<ROR4>(a[k]); a[k] += dpp<XOR8>(a[k]); }
    if ((lane & 12) == 0) *(f32x2*)(lds + L_QRED + w * 32 + 8 * c4 + 4 * (lane >> 5) + 2 * ((lane >> 4) & 1)) = f32x2{a[0], a[1]};
    tag = __builtin_amdgcn_readfirstlane(G) + 1u;
    asm volatile("s_waitcnt lgkmcnt(0)" ::: "memory");
    unsigned arr = 0;
    if (lane == 0) arr = __hip_atomic_fetch_add((LAS unsigned*)(lds + L_CNT), 1u, __ATOMIC_RELAXED, __HIP_MEMORY_SCOPE_WORKGROUP);
    arr = __builtin_amdgcn_readfirstlane(arr);
    asm volatile("" ::: "memory");
    STAMP(5);
    if (arr == 3 && lane < 8) {
      const float* qr = lds + L_QRED + 4 * lane;
      const f32x4 sq = (*(const f32x4*)qr + *(const f32x4*)(qr + 32)) + (*(const f32x4*)(qr + 64) + *(const f32x4*)(qr + 96));
      const u32x4 gr = {cvtpk(sq[0], sq[1]), tag, cvtpk(sq[2], sq[3]), tag};
      __builtin_amdgcn_raw_buffer_store_b128(gr, rs, WS_QG + j * 4096 + idx * 128 + lane * 16, 0, AUX_SC1);
    }
    if (mine) {
      u32x4 g = {0u, ~tag, 0u, ~tag};
      bool miss = true;
#pragma nounroll
      for (unsigned spins = 0; spins < SPIN_CAP; ++spins) {
        asm volatile("" ::: "memory");
        const u32x4 t = __builtin_amdgcn_raw_buffer_load_b128(rs, miss ? WS_QG + j * 4096 + w * 1024 + lane * 16 : WS_END, 0, AUX_SC1);
        if (miss) g = t;
        miss = ((g[1] ^ tag) | (g[3] ^ tag)) != 0;
        if (__builtin_amdgcn_ballot_w64(miss) == 0) break;
      }
      *(u32x2*)(lds + L_Q + w * 128 + lane * 2) = u32x2{g[0], g[2]};
    }
    STAMP(6);
    BARRIER();
  }
  unsigned wl[16], wh[16];
  {
    __builtin_amdgcn_s_sleep(WOSLEEP);
    const rsrc_t wr = __builtin_amdgcn_make_buffer_rsrc((void*)((myqh < 2 ? p.woA : p.woB) + (size_t)(hd * DH + sl * 32) * DM), 0, 32 * DM * 2, 0x00020000);
    const int cb = w * 256 + (lane & 15) * 16;
    const int wof = cb < DM * 2 ? (lane >> 4) * (DM * 2) + cb : 32 * DM * 2;
    u32x4 wt[8];
#pragma unroll
    for (int i = 0; i < 8; ++i) wt[i] = __builtin_amdgcn_raw_buffer_load_b128(wr, wof, i * 4 * DM * 2, AUX_NT);
    float* lw = lds + L_WO + w * 2048;
#pragma unroll
    for (int i = 0; i < 8; ++i) *(u32x4*)(lw + i * 256 + lane * 4) = wt[i];
    asm volatile("s_waitcnt lgkmcnt(0)" ::: "memory");
    unsigned wo[32];
#pragma unroll
    for (int i = 0; i < 32; ++i) wo[i] = __float_as_uint(lw[i * 64 + lane]);
#pragma unroll
    for (int i = 0; i < 16; ++i) { wl[i] = __builtin_amdgcn_perm(wo[2 * i + 1], wo[2 * i], 0x05040100u); wh[i] = __builtin_amdgcn_perm(wo[2 * i + 1], wo[2 * i], 0x07060302u); }
  }
  float gate;
  {
    const int l8 = lane < 56 ? 8 * lane : 0;
    u32x4 xa = *(const u32x4*)(p.x + l8); const u32x4 ga = *(const u32x4*)(p.gw + l8);
    if (lane >= 56) xa = u32x4{0u, 0u, 0u, 0u};
    float z = dot2(xa[0], ga[0], 0.f); z = dot2(xa[1], ga[1], z); z = dot2(xa[2], ga[2], z); z = dot2(xa[3], ga[3], z);
    z = rowsum16(z); z = tr16(z, z); z = tr32(z, z);
    gate = 1.f / (1.f + __expf(-z));
    if (myqh >= 2) gate = 1.f - gate;
  }
#pragma unroll
  for (int i = 0; i < 16; ++i) asm volatile("" : "+v"(wl[i]), "+v"(wh[i]));
  asm volatile("" : "+v"(gate));
  BARRIER();
  STAMP(7);
  if (mine) {
    const float* ml = lds + L_ML + w * 8;
    const f32x4 ma = *(const f32x4*)ml, mb = *(const f32x4*)(ml + 4);
    const float M = fmaxf(fmaxf(ma[0], ma[2]), fmaxf(mb[0], mb[2]));
    float e0 = __builtin_amdgcn_exp2f(ma[0] - M), e1 = __builtin_amdgcn_exp2f(ma[2] - M), e2 = __builtin_amdgcn_exp2f(mb[0] - M), e3 = __builtin_amdgcn_exp2f(mb[2] - M);
    const float L = e0 * ma[1] + e1 * ma[3] + e2 * mb[1] + e3 * mb[3];
    const float rl = __builtin_amdgcn_rcpf(L);
    const float* mp = lds + L_MRG + w * 4 * DH + 4 * lane;
    const f32x4 o = (e0 * *(const f32x4*)mp + e1 * *(const f32x4*)(mp + DH) + e2 * *(const f32x4*)(mp + 2 * DH) + e3 * *(const f32x4*)(mp + 3 * DH)) * rl;
    const unsigned lam = __float_as_uint(M + __builtin_amdgcn_logf(L)) >> ((lane & 1) * 16);
    const u32x4 ov = {cvtpk(o[0], o[1]), (tag << 8) | (lam & 0xffu), cvtpk(o[2], o[3]), (tag << 8) | ((lam >> 8) & 0xffu)};
    __builtin_amdgcn_raw_buffer_store_b128(ov, rs, WS_OB1 + ((j * 4 + w) * 12 + idx - pfirst) * 1024 + lane * 16, 0, AUX_SC1);
  }
  PollRegs P;
#pragma unroll
  for (int i = 0; i < 6; ++i) P.r[i] = u32x4{0u, 0u, 0u, 0u};
  const u32x4 rsw = {(unsigned)(u64)p.ws, (unsigned)((u64)p.ws >> 32) & 0xffffu, (unsigned)WS_END, 0x00020000u};
  if (w == 0) {
    const int np = myqh == 0 ? 12 : 11;
    const int ob = WS_OB1 + ((j * 4 + myqh) * 12 + (lane >> 3)) * 1024 + sl * 128 + (lane & 7) * 16;
    const bool has1 = (lane >> 3) + 8 < np;
    u32x4 g[2];
    gather2<8>(rs, rsw, ob, ob + 8192, true, has1, tag << 8, 8, g, P);
    STAMP(8);
    unsigned lb0 = __builtin_amdgcn_perm(g[0][3], g[0][1], 0x0c0c0400u), lb1 = __builtin_amdgcn_perm(g[1][3], g[1][1], 0x0c0c0400u);
    lb0 <<= (lane & 1) * 16; lb1 <<= (lane & 1) * 16;
    lb0 |= __builtin_amdgcn_mov_dpp(lb0, XOR1, 0xf, 0xf, true); lb1 |= __builtin_amdgcn_mov_dpp(lb1, XOR1, 0xf, 0xf, true);
    const float la0 = __uint_as_float(lb0), la1 = has1 ? __uint_as_float(lb1) : -INFINITY;
    float M = fmaxf(la0, la1);
    M = fmaxf(M, dpp<XOR8>(M)); M = mx16(M); M = mx32(M);
    const float e0 = __builtin_amdgcn_exp2f(la0 - M), e1 = __builtin_amdgcn_exp2f(la1 - M);
    float L = e0 + e1;
    float r0 = tr32(e0 * bflo(g[0][0]) + e1 * bflo(g[1][0]), e0 * bflo(g[0][2]) + e1 * bflo(g[1][2]));
    float r1 = tr32(e0 * bfhi(g[0][0]) + e1 * bfhi(g[1][0]), e0 * bfhi(g[0][2]) + e1 * bfhi(g[1][2]));
    L = tr32(L, L);
    r0 = tr16(r0, r0); r1 = tr16(r1, r1); L = tr16(L, L);
    r0 += dpp<XOR8>(r0); r1 += dpp<XOR8>(r1); L += dpp<XOR8>(L);
    const float inv = __builtin_amdgcn_rcpf(L);
    lds[L_O + 2 * (lane & 7) + (lane >> 5)] = __uint_as_float(cvtpk(r0 * inv, r1 * inv));
  }
  BARRIER();
  {
    float y0 = 0.f, y1 = 0.f;
#pragma unroll
    for (int i4 = 0; i4 < 4; ++i4) {
      const u32x4 sv = *(const u32x4*)(lds + L_O + 4 * i4);
#pragma unroll
      for (int k = 0; k < 4; ++k) { y0 = dot2(wl[4 * i4 + k], sv[k], y0); y1 = dot2(wh[4 * i4 + k], sv[k], y1); }
    }
    const u32x2 yv = {cvtpk(y0 * gate, y1 * gate), tag};
    __builtin_amdgcn_raw_buffer_store_b64(yv, rs, tid < 224 ? WS_OB2 + b * OB2_REC + tid * 8 : WS_END, 0, AUX_SC1);
  }
  STAMP(9);
  if (b < NFIN) {
    VMWAIT(0);
    KEEP(P);
    const int ob = WS_OB2 + (64 * w + (lane >> 1)) * OB2_REC + b * 32 + (lane & 1) * 16;
    u32x4 g[2];
    gather2<0>(rs, rsw, ob, ob + 32 * OB2_REC, true, true, tag, 32, g, P);
    STAMP(10);
    float r0 = tr32(bflo(g[0][0]) + bflo(g[1][0]), bflo(g[0][2]) + bflo(g[1][2])), r1 = tr32(bfhi(g[0][0]) + bfhi(g[1][0]), bfhi(g[0][2]) + bfhi(g[1][2]));
    r0 = tr16(r0, r1);
    r0 += dpp<XOR8>(r0); r0 += dpp<ROR4>(r0); r0 += dpp<XOR2>(r0);
    lds[L_FIN + (4 * (lane & 1) + 2 * (lane >> 5) + ((lane >> 4) & 1)) * 4 + w] = r0;
    asm volatile("s_waitcnt lgkmcnt(0)" ::: "memory");
    unsigned arr = 0;
    if (lane == 0) arr = __hip_atomic_fetch_add((LAS unsigned*)(lds + L_CNT + 1), 1u, __ATOMIC_RELAXED, __HIP_MEMORY_SCOPE_WORKGROUP);
    arr = __builtin_amdgcn_readfirstlane(arr);
    asm volatile("" ::: "memory");
    if (arr == 3) {
      if (lane < 4) {
        const f32x4 u = *(const f32x4*)(lds + L_FIN + 8 * lane), v = *(const f32x4*)(lds + L_FIN + 8 * lane + 4);
        *(unsigned*)(p.out + 8 * b + 2 * lane) = cvtpk((u[0] + u[1]) + (u[2] + u[3]), (v[0] + v[1]) + (v[2] + v[3]));
      }
      if (b == 0 && lane == 0) __hip_atomic_store((GAS unsigned*)(p.ws + WS_G), tag, __ATOMIC_RELAXED, __HIP_MEMORY_SCOPE_AGENT);
      STAMP(11);
      STAMP_FLUSH(10, 12);
    }
  }
  KEEP(P);
  if (w == 0) { STAMP_FLUSH(0, 1); STAMP_FLUSH(5, 6); STAMP_FLUSH(7, 10); }
  if (mine) STAMP_FLUSH(6, 7);
}

extern "C" void kernel_launch(void* const* d_in, const int* in_sizes, int n_in, void* d_out, int out_size, void* d_ws, size_t ws_size, hipStream_t stream) {
  Params p;
  p.x = (const u16*)d_in[0];
  p.kA = (const u16*)d_in[1]; p.vA = (const u16*)d_in[2]; p.wqA = (const u16*)d_in[3]; p.woA = (const u16*)d_in[4];
  p.kB = (const u16*)d_in[6]; p.vB = (const u16*)d_in[7]; p.wqB = (const u16*)d_in[8]; p.woB = (const u16*)d_in[9];
  p.gw = (const u16*)d_in[10];
  p.out = (u16*)d_out; p.ws = (unsigned char*)d_ws;
  k_fused<<<NWG, NTHR, 0, stream>>>(p);
}
```

```cpp
#include <hip/hip_runtime.h>
#include <hip/hip_bf16.h>
#include <stdint.h>

typedef unsigned u32x4 __attribute__((ext_vector_type(4)));
typedef unsigned u32x2 __attribute__((ext_vector_type(2)));
typedef float f32x4 __attribute__((ext_vector_type(4)));
typedef float f32x2 __attribute__((ext_vector_type(2)));
typedef __bf16 bf16x2 __attribute__((ext_vector_type(2)));
typedef unsigned short u16;
typedef unsigned long long u64;
typedef __amdgpu_buffer_rsrc_t rsrc_t;
#define GAS __attribute__((address_space(1)))
#define LAS __attribute__((address_space(3)))

constexpr int DM = 448, DH = 256, SK = 8192, NQ = 4096;
constexpr int NWG = 256, NTHR = 512, NFIN = 56;
#define PACE 6
#define GATE 7
#define TAU 112
#define TOFF 150
#define GAIN 4
#define D0 7
#define DMIN 5
#define DMAX 10
#define QDELAY 48
#define DEL1 18
#define DEL2 26
constexpr int WS_G = 0;
constexpr int WS_T0 = 1024;
constexpr int WS_QG = 4096;
constexpr int WS_OB1 = WS_QG + 131072;
constexpr int WS_IB2 = WS_OB1 + 32 * 12 * 1024;
constexpr int WS_END = WS_IB2 + NFIN * 8192;
constexpr unsigned SPIN_CAP = 1u << 18;
constexpr int AUX_SC1 = 16, AUX_NT = 2, AUX_SYS = 17, RSRC3 = 0x00020000;
constexpr int L_MRG = 0;
constexpr int L_ML = 16 * DH;
constexpr int L_QRED = L_ML + 32;
constexpr int L_Q = L_QRED + 256;
constexpr int L_CNT = L_Q + 512;
constexpr int L_O = L_CNT + 4;
constexpr int L_FIN = L_O + 16;
constexpr int L_WO = L_FIN + 32;
constexpr int L_END = L_WO + 4 * 2048;

#define STAMP(i) do { } while (0)
#define STAMP_FLUSH(lo, hi) do { } while (0)
#define TSTARG
#define TSTPAR

__device__ __forceinline__ float dot2(unsigned a, unsigned b, float acc) {
  return __builtin_amdgcn_fdot2_f32_bf16(__builtin_bit_cast(bf16x2, a), __builtin_bit_cast(bf16x2, b), acc, false);
}
__device__ __forceinline__ float bflo(unsigned u) { return __builtin_bit_cast(float, u << 16); }
__device__ __forceinline__ float bfhi(unsigned u) { return __builtin_bit_cast(float, u & 0xffff0000u); }
__device__ __forceinline__ unsigned cvtpk(float lo, float hi) { return __builtin_bit_cast(unsigned, __builtin_convertvector(f32x2{lo, hi}, bf16x2)); }
__device__ __forceinline__ float vmax(float a, float b) { return __builtin_elementwise_maximum(a, b); }
template <int CTRL> __device__ __forceinline__ unsigned dppu(unsigned x) { return (unsigned)__builtin_amdgcn_mov_dpp((int)x, CTRL, 0xf, 0xf, true); }
template <int CTRL> __device__ __forceinline__ float dpp(float x) { return __uint_as_float(dppu<CTRL>(__float_as_uint(x))); }
constexpr int XOR1 = 0xB1, XOR2 = 0x4E, XOR7 = 0x141, XOR8 = 0x128, ROR4 = 0x124;
__device__ __forceinline__ float tr32(float a, float b) {
  auto t = __builtin_amdgcn_permlane32_swap(__float_as_uint(a), __float_as_uint(b), false, false);
  return __uint_as_float(t[0]) + __uint_as_float(t[1]);
}
__device__ __forceinline__ float tr16(float a, float b) {
  auto t = __builtin_amdgcn_permlane16_swap(__float_as_uint(a), __float_as_uint(b), false, false);
  return __uint_as_float(t[0]) + __uint_as_float(t[1]);
}
__device__ __forceinline__ float mx32(float a) {
  auto t = __builtin_amdgcn_permlane32_swap(__float_as_uint(a), __float_as_uint(a), false, false);
  return vmax(__uint_as_float(t[0]), __uint_as_float(t[1]));
}
__device__ __forceinline__ float mx16(float a) {
  auto t = __builtin_amdgcn_permlane16_swap(__float_as_uint(a), __float_as_uint(a), false, false);
  return vmax(__uint_as_float(t[0]), __uint_as_float(t[1]));
}
__device__ __forceinline__ float rowsum16(float x) { x += dpp<XOR1>(x); x += dpp<XOR2>(x); x += dpp<XOR7>(x); x += dpp<XOR8>(x); return x; }
__device__ __forceinline__ u32x4 sel4(bool c, u32x4 a, u32x4 b) { return u32x4{c ? a[0] : b[0], c ? a[1] : b[1], c ? a[2] : b[2], c ? a[3] : b[3]}; }
__device__ __forceinline__ unsigned now32() { return (unsigned)__builtin_amdgcn_s_memrealtime(); }
#define SCHED() __builtin_amdgcn_sched_barrier(0)
#define BARRIER() do { SCHED(); asm volatile("s_waitcnt lgkmcnt(0)" ::: "memory"); __builtin_amdgcn_s_barrier(); asm volatile("" ::: "memory"); SCHED(); } while (0)
#define VMWAIT(n) asm volatile("s_waitcnt vmcnt(%0)" :: "n"(n) : "memory")

template <int SH>
__device__ __forceinline__ void gather2(rsrc_t rs, int o0, int o1, bool m1, unsigned tagw, int few, u32x4 (&g)[2]) {
  g[0] = u32x4{0u, 0u, 0u, 0u}; g[1] = g[0];
  bool m0 = true;
#pragma nounroll
  for (unsigned spins = 0; spins < SPIN_CAP; ++spins) {
    asm volatile("" ::: "memory");
    u32x4 a = __builtin_amdgcn_raw_buffer_load_b128(rs, m0 ? o0 : WS_END, 0, AUX_SC1), c = __builtin_amdgcn_raw_buffer_load_b128(rs, m1 ? o1 : WS_END, 0, AUX_SC1);
    asm volatile("" : "+v"(a), "+v"(c));
    g[0] = sel4(m0, a, g[0]); g[1] = sel4(m1, c, g[1]);
    m0 = m0 & ((((a[1] ^ tagw) | (a[3] ^ tagw)) >> SH) != 0); m1 = m1 & ((((c[1] ^ tagw) | (c[3] ^ tagw)) >> SH) != 0);
    const int left = __builtin_popcountll(__builtin_amdgcn_ballot_w64(m0)) + __builtin_popcountll(__builtin_amdgcn_ballot_w64(m1));
    if (left == 0) break;
    if (left > few) __builtin_amdgcn_s_sleep(4);
  }
}

template <int LO, int HI>
__device__ __forceinline__ void gate_rt(int d) {
  asm volatile(
      "s_waitcnt vmcnt(%1)\n\t"
      "s_cmp_gt_i32 %0, %1\n\ts_cbranch_scc1 .Lg%=\n\t"
      "s_waitcnt vmcnt(%2)\n\t"
      "s_cmp_gt_i32 %0, %2\n\ts_cbranch_scc1 .Lg%=\n\t"
      "s_waitcnt vmcnt(%3)\n\t"
      "s_cmp_gt_i32 %0, %3\n\ts_cbranch_scc1 .Lg%=\n\t"
      "s_waitcnt vmcnt(%4)\n\t"
      "s_cmp_gt_i32 %0, %4\n\ts_cbranch_scc1 .Lg%=\n\t"
      "s_waitcnt vmcnt(%5)\n\t"
      ".Lg%=:" :: "s"(d), "n"(HI - 1), "n"(LO + 2), "n"(LO + 1), "n"(LO), "n"(LO - 1) : "scc", "memory");
}

struct Params {
  const u16 *x, *kA, *vA, *wqA, *woA, *kB, *vB, *wqB, *woB, *gw;
  u16* out;
  unsigned char* ws;
};
struct St { float m[2], l[2]; f32x2 acc[2][4]; };

enum { NONE, FREE, FIXED, PACED, LAST };
template <int NH, int MODE>
__device__ __forceinline__ void do_chunk(u32x4 (&d)[8], const u32x4 (&qv)[2], St& s, rsrc_t kr, rsrc_t vr, int voff, int so, int dg = 0, float* mlp = nullptr) {
  constexpr float C2 = 0.0625f * 1.4426950408889634f;
  auto refill = [&](int i) {
    if (MODE == FIXED) VMWAIT(GATE + NH - 2);
    if (MODE == PACED) gate_rt<DMIN + NH - 1, DMAX + NH - 1>(dg);
    if (MODE == LAST) d[i] = __builtin_amdgcn_raw_buffer_load_b128(kr, voff, i * 4 * DM * 2, AUX_NT);
    else if (MODE != NONE) d[i] = __builtin_amdgcn_raw_buffer_load_b128(i < 4 ? kr : vr, voff + (i & 3) * 1024, so, AUX_NT);
  };
  float sc[NH][4];
#pragma unroll
  for (int i = 0; i < 4; ++i) {
#pragma unroll
    for (int h = 0; h < NH; ++h) { float t = dot2(d[i][0], qv[h][0], 0.f); t = dot2(d[i][1], qv[h][1], t); t = dot2(d[i][2], qv[h][2], t); sc[h][i] = dot2(d[i][3], qv[h][3], t); }
    if (MODE != NONE) {
#pragma unroll
      for (int h = 0; h < NH; ++h) asm volatile("" : "+v"(sc[h][i]));
      refill(i);
      SCHED();
    }
  }
  float pr[NH][4];
#pragma unroll
  for (int h = 0; h < NH; ++h) {
    float a = rowsum16(tr16(sc[h][0], sc[h][1])) * C2, b = rowsum16(tr16(sc[h][2], sc[h][3])) * C2;
    float mn = vmax(mx16(vmax(a, b)), s.m[h]);
    if (MODE == LAST) mn = mx32(mn);
    const float al = __builtin_amdgcn_exp2f(s.m[h] - mn);
    a = __builtin_amdgcn_exp2f(a - mn); b = __builtin_amdgcn_exp2f(b - mn);
    s.l[h] = fmaf(s.l[h], al, a + b);
    s.m[h] = mn;
    auto ta = __builtin_amdgcn_permlane16_swap(__float_as_uint(a), __float_as_uint(a), false, false);
    auto tb = __builtin_amdgcn_permlane16_swap(__float_as_uint(b), __float_as_uint(b), false, false);
    pr[h][0] = __uint_as_float(ta[0]); pr[h][1] = __uint_as_float(ta[1]); pr[h][2] = __uint_as_float(tb[0]); pr[h][3] = __uint_as_float(tb[1]);
    const f32x2 al2 = {al, al};
#pragma unroll
    for (int k = 0; k < 4; ++k) s.acc[h][k] *= al2;
    if (MODE == LAST) { float L = tr16(s.l[h], s.l[h]); L = tr32(L, L); *(f32x2*)(mlp + 8 * h) = f32x2{mn, L}; }
  }
  if (MODE == LAST) BARRIER();
#pragma unroll
  for (int i = 0; i < 4; ++i) {
    f32x2 vf[4];
#pragma unroll
    for (int k = 0; k < 4; ++k) vf[k] = f32x2{bflo(d[4 + i][k]), bfhi(d[4 + i][k])};
#pragma unroll
    for (int h = 0; h < NH; ++h) {
      const f32x2 pp = f32x2{pr[h][i], pr[h][i]};
#pragma unroll
      for (int k = 0; k < 4; ++k) s.acc[h][k] = __builtin_elementwise_fma(pp, vf[k], s.acc[h][k]);
      asm volatile("" : "+v"(s.acc[h][0]), "+v"(s.acc[h][1]), "+v"(s.acc[h][2]), "+v"(s.acc[h][3]));
    }
    refill(4 + i);
    SCHED();
  }
}

template <int NH, bool FINAL>
__device__ __forceinline__ void flush(float* lds, int slot, int lane, St& s) {
#pragma unroll
  for (int h = 0; h < NH; ++h) {
    if (!FINAL) {
      const float M = mx32(s.m[h]);
      const float e = __builtin_amdgcn_exp2f(s.m[h] - M);
      float L = s.l[h] * e; L = tr16(L, L); L = tr32(L, L);
      *(f32x2*)(lds + L_ML + (slot + 4 * h) * 2) = f32x2{M, L};
#pragma unroll
      for (int k = 0; k < 4; ++k) s.acc[h][k] *= f32x2{e, e};
    }
    f32x4 o;
    o[0] = tr32(s.acc[h][0][0], s.acc[h][2][0]); o[1] = tr32(s.acc[h][0][1], s.acc[h][2][1]);
    o[2] = tr32(s.acc[h][1][0], s.acc[h][3][0]); o[3] = tr32(s.acc[h][1][1], s.acc[h][3][1]);
    *(f32x4*)(lds + L_MRG + (slot + 4 * h) * DH + 8 * (lane & 31) + 4 * (lane >> 5)) = o;
    s.m[h] = -INFINITY; s.l[h] = 0.f;
#pragma unroll
    for (int k = 0; k < 4; ++k) s.acc[h][k] = f32x2{0.f, 0.f};
  }
}

__device__ __forceinline__ void finish(const Params& p, float* lds, int w4, int lane, const u32x4 (&wt)[8] TSTPAR) {
  STAMP(12);
  BARRIER();
  float* lw = lds + L_WO + w4 * 2048;
#pragma unroll
  for (int i = 0; i < 8; ++i) *(u32x4*)(lw + i * 256 + lane * 4) = wt[i];
  const unsigned tag = __builtin_amdgcn_readfirstlane(*(LAS unsigned*)(lds + L_CNT + 2));
  asm volatile("s_waitcnt lgkmcnt(0)" ::: "memory");
  const int pl = 2 * (lane & 31) + (lane >> 5);
  unsigned wl[16], wh[16];
#pragma unroll
  for (int i = 0; i < 16; ++i) {
    const unsigned a = __float_as_uint(lw[2 * i * 64 + pl]), c = __float_as_uint(lw[(2 * i + 1) * 64 + pl]);
    wl[i] = __builtin_amdgcn_perm(c, a, 0x05040100u); wh[i] = __builtin_amdgcn_perm(c, a, 0x07060302u);
  }
#pragma unroll
  for (int i = 0; i < 16; ++i) asm volatile("" : "+v"(wl[i]), "+v"(wh[i]));
  const rsrc_t rs = __builtin_amdgcn_make_buffer_rsrc((void*)p.ws, 0, WS_END, RSRC3);
  const int cp = w4 * 64 + pl;
  int yo = (cp < DM / 2 && lane < 32) ? WS_IB2 + (cp >> 2) * 8192 + blockIdx.x * 32 + (cp & 2) * 8 : WS_END;
  asm volatile("" : "+v"(yo));
  STAMP(3);
  BARRIER();
  float y0 = 0.f, y1 = 0.f, y2 = 0.f, y3 = 0.f;
#pragma unroll
  for (int i4 = 0; i4 < 4; ++i4) {
    const u32x4 sv = *(const u32x4*)(lds + L_O + 4 * i4);
    y0 = dot2(wl[4 * i4], sv[0], y0); y1 = dot2(wh[4 * i4], sv[0], y1); y2 = dot2(wl[4 * i4 + 1], sv[1], y2); y3 = dot2(wh[4 * i4 + 1], sv[1], y3);
    y0 = dot2(wl[4 * i4 + 2], sv[2], y0); y1 = dot2(wh[4 * i4 + 2], sv[2], y1); y2 = dot2(wl[4 * i4 + 3], sv[3], y2); y3 = dot2(wh[4 * i4 + 3], sv[3], y3);
  }
  const unsigned yp = cvtpk(y0 + y2, y1 + y3);
  const auto ys = __builtin_amdgcn_permlane32_swap(yp, yp, false, false);
  __builtin_amdgcn_raw_buffer_store_b128(u32x4{ys[0], tag, ys[1], tag}, rs, yo, 0, AUX_SC1);
  STAMP(9);
}

__device__ __forceinline__ int gchunk(int idx, int w4, int n) { return idx * 96 + (n >> 3) * 32 + (((n + idx) & 7) << 2) + w4; }

template <bool FIRSTB>
__device__ __forceinline__ void stream(const Params& p, float* lds, int j, int idx, int w4, int lane TSTPAR) {
  const int voff = lane * 16;
  const rsrc_t kB = __builtin_amdgcn_make_buffer_rsrc((void*)(p.kB + (size_t)j * SK * DH), 0, SK * DH * 2, RSRC3);
  const rsrc_t vB = __builtin_amdgcn_make_buffer_rsrc((void*)(p.vB + (size_t)j * SK * DH), 0, SK * DH * 2, RSRC3);
  const rsrc_t kA = __builtin_amdgcn_make_buffer_rsrc((void*)(p.kA + (size_t)2 * j * SK * DH), 0, 2 * SK * DH * 2, RSRC3);
  const rsrc_t vA = __builtin_amdgcn_make_buffer_rsrc((void*)(p.vA + (size_t)2 * j * SK * DH), 0, 2 * SK * DH * 2, RSRC3);
  const rsrc_t k0 = FIRSTB ? kB : kA, v0 = FIRSTB ? vB : vA;
  constexpr int C0 = FIRSTB ? 0 : 1024, NH0 = FIRSTB ? 2 : 1;
  const int myqh = idx >> 3;
  const rsrc_t wr = __builtin_amdgcn_make_buffer_rsrc((void*)((myqh < 2 ? p.woA : p.woB) + (size_t)((2 * j + (myqh & 1)) * DH + (idx & 7) * 32) * DM), 0, 32 * DM * 2, RSRC3);
  const int cb = w4 * 256 + (lane & 15) * 16;
  const int wof = cb < DM * 2 ? (lane >> 4) * (DM * 2) + cb : 32 * DM * 2;
  u32x4 buf[4][8];
  BARRIER();
#pragma unroll
  for (int n = 0; n < 32; ++n) {
    if (n == 0) STAMP(13);
    if (n >= PACE) VMWAIT(PACE - 1);
    if (n == PACE) STAMP(14);
    buf[n >> 3][n & 7] = __builtin_amdgcn_raw_buffer_load_b128((n & 7) < 4 ? k0 : v0, voff + (n & 3) * 1024, (gchunk(idx, w4, n >> 3) - C0) * 4096, AUX_NT);
    SCHED();
  }
  STAMP(1);
  BARRIER();
  STAMP(2);
  unsigned due = __builtin_amdgcn_readfirstlane(*(LAS unsigned*)(lds + L_CNT + 3)) + TOFF + 5 * TAU;
  auto depth = [&](int extra) { due += TAU; return D0 + extra + (((int)(now32() - due) * GAIN) >> 8); };
  u32x4 qv[2];
  St s;
#pragma unroll
  for (int h = 0; h < 2; ++h) {
    s.m[h] = -INFINITY; s.l[h] = 0.f;
#pragma unroll
    for (int k = 0; k < 4; ++k) s.acc[h][k] = f32x2{0.f, 0.f};
  }
  int qh = FIRSTB ? 2 : (idx <= 21 ? 0 : 1);
  qv[0] = *(const u32x4*)(lds + L_Q + qh * 128 + 4 * (lane & 31));
  qv[1] = *(const u32x4*)(lds + L_Q + 3 * 128 + 4 * (lane & 31));
  do_chunk<NH0, FREE>(buf[0], qv, s, k0, v0, voff, (gchunk(idx, w4, 4) - C0) * 4096);
  do_chunk<NH0, NONE>(buf[1], qv, s, k0, v0, voff, 0);
  do_chunk<NH0, FIXED>(buf[2], qv, s, k0, v0, voff, (gchunk(idx, w4, 5) - C0) * 4096);
  do_chunk<NH0, NONE>(buf[3], qv, s, k0, v0, voff, 0);
  STAMP(17);
  int n = 4;
  if (FIRSTB) {
    const int nb = idx < 10 ? 22 : 16;
#pragma nounroll
    for (; n < nb; n += 2) {
      const int c = gchunk(idx, w4, n + 2), c2 = gchunk(idx, w4, n + 3);
      const bool isb = c < 1024;
      const rsrc_t kr = isb ? kB : kA, vr = isb ? vB : vA;
      do_chunk<2, PACED>(buf[0], qv, s, kr, vr, voff, (c & 1023) * 4096, depth(1));
      do_chunk<2, PACED>(buf[2], qv, s, kr, vr, voff, (c2 & 1023) * 4096, depth(1));
    }
    if (idx < 10) {
      STAMP(18);
      do_chunk<2, NONE>(buf[0], qv, s, wr, wr, wof, 0);
      do_chunk<2, LAST>(buf[2], qv, s, wr, wr, wof, 0, 0, lds + L_ML + (8 + w4) * 2);
      STAMP(19);
      flush<2, true>(lds, 8 + w4, lane, s);
      finish(p, lds, w4, lane, buf[2] TSTARG);
      return;
    }
    flush<2, false>(lds, 8 + w4, lane, s);
    qh = 0;
    qv[0] = *(const u32x4*)(lds + L_Q + 4 * (lane & 31));
  }
#pragma nounroll
  for (; n < 22; n += 2) {
    if (!FIRSTB && idx == 21 && n == 8) {
      flush<1, false>(lds, w4, lane, s);
      qh = 1;
      qv[0] = *(const u32x4*)(lds + L_Q + 128 + 4 * (lane & 31));
    }
    do_chunk<1, PACED>(buf[0], qv, s, kA, vA, voff, (gchunk(idx, w4, n + 2) - 1024) * 4096, depth(0));
    do_chunk<1, PACED>(buf[2], qv, s, kA, vA, voff, (gchunk(idx, w4, n + 3) - 1024) * 4096, depth(0));
  }
  STAMP(18);
  do_chunk<1, NONE>(buf[0], qv, s, wr, wr, wof, 0);
  do_chunk<1, LAST>(buf[2], qv, s, wr, wr, wof, 0, 0, lds + L_ML + (qh * 4 + w4) * 2);
  STAMP(19);
  flush<1, true>(lds, qh * 4 + w4, lane, s);
  finish(p, lds, w4, lane, buf[2] TSTARG);
}

__global__ void __launch_bounds__(NTHR, 2) k_fused(Params p) {
  __shared__ __attribute__((aligned(16))) float lds[L_END];
  const int b = blockIdx.x, j = b & 7, idx = b >> 3;
  const int tid = threadIdx.x, lane = tid & 63, w = __builtin_amdgcn_readfirstlane(tid >> 6);
  asm volatile("" :: "s"(p.x), "s"(p.kA), "s"(p.vA), "s"(p.wqA), "s"(p.woA), "s"(p.kB), "s"(p.vB), "s"(p.wqB), "s"(p.woB), "s"(p.gw), "s"(p.out), "s"(p.ws));
  if (w >= 4) {
    if (idx <= 10) { stream<true>(p, lds, j, idx, w - 4, lane TSTARG); if (w == 4) { STAMP_FLUSH(1, 4); STAMP_FLUSH(9, 10); STAMP_FLUSH(12, 20); } return; }
    stream<false>(p, lds, j, idx, w - 4, lane TSTARG);
    if (w == 4) { STAMP_FLUSH(1, 4); STAMP_FLUSH(9, 10); STAMP_FLUSH(12, 20); }
    return;
  }
  const rsrc_t rs = __builtin_amdgcn_make_buffer_rsrc((void*)p.ws, 0, WS_END, RSRC3);
  const int myqh = idx >> 3, sl = idx & 7, hd = 2 * j + (myqh & 1);
  const int pfirst = w == 0 ? 10 : (w == 1 ? 21 : 0), plast = w == 0 ? 21 : (w == 1 ? 31 : 10);
  const bool mine = idx >= pfirst && idx <= plast;
  const unsigned t_in = now32();
  unsigned tag;
  {
    const bool act = tid < 224;
    const int r0 = (sl & 1) * 224 + ((act ? tid : 0) >> 3) * 8, c8 = tid & 7;
    const u16* wq = (myqh < 2 ? p.wqA : p.wqB) + (size_t)r0 * NQ + hd * DH + (sl >> 1) * 64 + 8 * c8;
    u32x4 wv[8];
#pragma unroll
    for (int i = 0; i < 8; ++i) wv[i] = __builtin_nontemporal_load((const u32x4*)(wq + (size_t)i * NQ));
    u32x4 xv = *(const u32x4*)(p.x + r0);
    const unsigned G = __hip_atomic_load((GAS unsigned*)(p.ws + WS_G), __ATOMIC_RELAXED, __HIP_MEMORY_SCOPE_AGENT);
    if (tid < 2) *(LAS unsigned*)(lds + L_CNT + tid) = 0u;
    BARRIER();
    if (!act) xv = u32x4{0u, 0u, 0u, 0u};
    float a[8];
#pragma unroll
    for (int k = 0; k < 4; ++k) {
      float lo = 0.f, hi = 0.f;
#pragma unroll
      for (int i = 0; i < 4; ++i) {
        lo = dot2(__builtin_amdgcn_perm(wv[2 * i + 1][k], wv[2 * i][k], 0x05040100u), xv[i], lo);
        hi = dot2(__builtin_amdgcn_perm(wv[2 * i + 1][k], wv[2 * i][k], 0x07060302u), xv[i], hi);
      }
      a[2 * k] = act ? lo : 0.f; a[2 * k + 1] = act ? hi : 0.f;
    }
#pragma unroll
    for (int k = 0; k < 4; ++k) a[k] = tr32(a[k], a[k + 4]);
#pragma unroll
    for (int k = 0; k < 2; ++k) a[k] = tr16(a[k], a[k + 2]);
#pragma unroll
    for (int k = 0; k < 2; ++k) a[k] += dpp<XOR8>(a[k]);
    if ((lane & 8) == 0) *(f32x2*)(lds + L_QRED + w * 64 + 8 * c8 + 4 * (lane >> 5) + 2 * ((lane >> 4) & 1)) = f32x2{a[0], a[1]};
    tag = __builtin_amdgcn_readfirstlane(G) + 1u;
    if (tid == 0) *(LAS unsigned*)(lds + L_CNT + 2) = tag;
    asm volatile("s_waitcnt lgkmcnt(0)" :: "s"(tag) : "memory");
    unsigned arr = 0;
    if (lane == 0) arr = __hip_atomic_fetch_add((LAS unsigned*)(lds + L_CNT), 1u, __ATOMIC_RELAXED, __HIP_MEMORY_SCOPE_WORKGROUP);
    arr = __builtin_amdgcn_readfirstlane(arr);
    asm volatile("" ::: "memory");
    if (b == 0 && w == 3) __builtin_amdgcn_raw_buffer_store_b128(u32x4{t_in, tag, t_in, tag}, rs, lane == 0 ? WS_T0 : WS_END, 0, AUX_SC1);
    if (arr == 3 && lane < 32) {
      const float* qr = lds + L_QRED + 2 * lane;
      const f32x2 sq = (*(const f32x2*)qr + *(const f32x2*)(qr + 64)) + (*(const f32x2*)(qr + 128) + *(const f32x2*)(qr + 192));
      __builtin_amdgcn_raw_buffer_store_b128(u32x4{__float_as_uint(sq[0]), tag, __float_as_uint(sq[1]), tag}, rs, WS_QG + j * 16384 + idx * 512 + lane * 16, 0, AUX_SC1);
    }
    __builtin_amdgcn_s_sleep(QDELAY);
    if (mine) {
      const int qo = WS_QG + j * 16384 + w * 4096 + (lane >> 4) * 1024 + (lane & 15) * 32;
      u32x4 g0, g1, g2, g3;
#pragma nounroll
      for (unsigned spins = 0; spins < SPIN_CAP; ++spins) {
        asm volatile("" ::: "memory");
        g0 = __builtin_amdgcn_raw_buffer_load_b128(rs, qo, 0, AUX_SC1); g1 = __builtin_amdgcn_raw_buffer_load_b128(rs, qo + 16, 0, AUX_SC1);
        g2 = __builtin_amdgcn_raw_buffer_load_b128(rs, qo + 512, 0, AUX_SC1); g3 = __builtin_amdgcn_raw_buffer_load_b128(rs, qo + 528, 0, AUX_SC1);
        const unsigned bad = (g0[1] ^ tag) | (g0[3] ^ tag) | (g1[1] ^ tag) | (g1[3] ^ tag) | (g2[1] ^ tag) | (g2[3] ^ tag) | (g3[1] ^ tag) | (g3[3] ^ tag);
        if (__builtin_amdgcn_ballot_w64(bad != 0) == 0) break;
        __builtin_amdgcn_s_sleep(8);
      }
      *(u32x2*)(lds + L_Q + w * 128 + lane * 2) = u32x2{cvtpk(__uint_as_float(g0[0]) + __uint_as_float(g2[0]), __uint_as_float(g0[2]) + __uint_as_float(g2[2])),
                                                        cvtpk(__uint_as_float(g1[0]) + __uint_as_float(g3[0]), __uint_as_float(g1[2]) + __uint_as_float(g3[2]))};
    } else if (w == (idx <= 10 ? 1 : 3)) {
      const u32x4 e = __builtin_amdgcn_raw_buffer_load_b128(rs, lane == 0 ? WS_T0 : WS_END, 0, AUX_SC1);
      if (lane == 0) *(LAS unsigned*)(lds + L_CNT + 3) = (e[1] == tag && e[3] == tag) ? e[0] : t_in;
    }
    BARRIER();
  }
  float gate;
  {
    const int l8 = lane < 56 ? 8 * lane : 0;
    const u32x4 xa = *(const u32x4*)(p.x + l8), ga = *(const u32x4*)(p.gw + l8);
    float z = dot2(xa[0], ga[0], 0.f); z = dot2(xa[1], ga[1], z); z = dot2(xa[2], ga[2], z); z = dot2(xa[3], ga[3], z);
    z = lane < 56 ? z : 0.f;
    z = rowsum16(z); z = tr16(z, z); z = tr32(z, z);
    gate = 1.f / (1.f + __expf(myqh < 2 ? -z : z));
    asm volatile("" : "+v"(gate));
  }
  BARRIER();
  f32x2 e0, e1, e2, e3; unsigned tw1, tw3; int pof; const float* mp = lds + L_MRG + w * 4 * DH + 4 * lane;
  {
    const float* ml = lds + L_ML + w * 8;
    const f32x4 ma = *(const f32x4*)ml, mb = *(const f32x4*)(ml + 4);
    const float M = vmax(vmax(ma[0], ma[2]), vmax(mb[0], mb[2]));
    const float a0 = __builtin_amdgcn_exp2f(ma[0] - M), a1 = __builtin_amdgcn_exp2f(ma[2] - M), a2 = __builtin_amdgcn_exp2f(mb[0] - M), a3 = __builtin_amdgcn_exp2f(mb[2] - M);
    const float L = a0 * ma[1] + a1 * ma[3] + a2 * mb[1] + a3 * mb[3];
    const float rl = __builtin_amdgcn_rcpf(L);
    e0 = f32x2{a0 * rl, a0 * rl}; e1 = f32x2{a1 * rl, a1 * rl}; e2 = f32x2{a2 * rl, a2 * rl}; e3 = f32x2{a3 * rl, a3 * rl};
    const unsigned lam = __float_as_uint(M + __builtin_amdgcn_logf(L)) >> ((lane & 1) * 16);
    tw1 = (tag << 8) | (lam & 0xffu); tw3 = (tag << 8) | ((lam >> 8) & 0xffu);
    pof = mine ? WS_OB1 + ((j * 4 + w) * 12 + idx - pfirst) * 1024 + lane * 16 : WS_END;
    asm volatile("" : "+v"(e0), "+v"(e1), "+v"(e2), "+v"(e3), "+v"(tw1), "+v"(tw3), "+v"(pof));
  }
  BARRIER();
  STAMP(7);
  {
    const f32x4 c0 = *(const f32x4*)mp, c1 = *(const f32x4*)(mp + DH), c2 = *(const f32x4*)(mp + 2 * DH), c3 = *(const f32x4*)(mp + 3 * DH);
    SCHED();
    const f32x2 lo = (e0 * c0.xy + e1 * c1.xy) + (e2 * c2.xy + e3 * c3.xy), hi = (e0 * c0.zw + e1 * c1.zw) + (e2 * c2.zw + e3 * c3.zw);
    __builtin_amdgcn_raw_buffer_store_b128(u32x4{cvtpk(lo[0], lo[1]), tw1, cvtpk(hi[0], hi[1]), tw3}, rs, pof, 0, AUX_SC1);
    SCHED();
    STAMP(6);
  }
  if (w == 0) {
    const int ob = WS_OB1 + ((j * 4 + myqh) * 12 + (lane >> 3)) * 1024 + sl * 128 + (lane & 7) * 16;
    const bool has1 = (lane >> 3) + 8 < (myqh == 0 ? 12 : 11);
    u32x4 g[2];
    __builtin_amdgcn_s_sleep(DEL1);
    gather2<8>(rs, ob, ob + 8192, has1, tag << 8, 8, g);
    STAMP(8);
    unsigned lb0 = __builtin_amdgcn_perm(g[0][3], g[0][1], 0x0c0c0400u), lb1 = __builtin_amdgcn_perm(g[1][3], g[1][1], 0x0c0c0400u);
    lb0 <<= (lane & 1) * 16; lb1 <<= (lane & 1) * 16;
    lb0 |= dppu<XOR1>(lb0); lb1 |= dppu<XOR1>(lb1);
    const float la0 = __uint_as_float(lb0), la1 = has1 ? __uint_as_float(lb1) : -INFINITY;
    float M = vmax(la0, la1);
    M = vmax(M, dpp<XOR8>(M)); M = mx16(M); M = mx32(M);
    const float f0 = __builtin_amdgcn_exp2f(la0 - M), f1 = __builtin_amdgcn_exp2f(la1 - M);
    float L = f0 + f1;
    float r0 = tr32(f0 * bflo(g[0][0]) + f1 * bflo(g[1][0]), f0 * bflo(g[0][2]) + f1 * bflo(g[1][2]));
    float r1 = tr32(f0 * bfhi(g[0][0]) + f1 * bfhi(g[1][0]), f0 * bfhi(g[0][2]) + f1 * bfhi(g[1][2]));
    L = tr32(L, L);
    r0 = tr16(r0, r0); r1 = tr16(r1, r1); L = tr16(L, L);
    r0 += dpp<XOR8>(r0); r1 += dpp<XOR8>(r1); L += dpp<XOR8>(L);
    const float inv = gate * __builtin_amdgcn_rcpf(L);
    lds[L_O + 2 * (lane & 7) + (lane >> 5)] = __uint_as_float(cvtpk(r0 * inv, r1 * inv));
  }
  BARRIER();
  if (b < NFIN) {
    const int ob = WS_IB2 + b * 8192 + w * 2048 + lane * 16;
    u32x4 g[2];
    __builtin_amdgcn_s_sleep(DEL2);
    gather2<0>(rs, ob, ob + 1024, true, tag, 16, g);
    STAMP(10);
    float r0 = tr32(bflo(g[0][0]) + bflo(g[1][0]), bflo(g[0][2]) + bflo(g[1][2])), r1 = tr32(bfhi(g[0][0]) + bfhi(g[1][0]), bfhi(g[0][2]) + bfhi(g[1][2]));
    r0 = tr16(r0, r1);
    r0 += dpp<XOR8>(r0); r0 += dpp<ROR4>(r0); r0 += dpp<XOR2>(r0);
    *(volatile LAS float*)(lds + L_FIN + (4 * (lane & 1) + 2 * (lane >> 5) + ((lane >> 4) & 1)) * 4 + w) = r0;
    unsigned arr; f32x4 u, v;
    {
      const unsigned ca = (unsigned)(size_t)(LAS float*)(lds + (lane == 0 ? L_CNT + 1 : L_QRED + lane)), fa = (unsigned)(size_t)(LAS float*)(lds + L_FIN + 8 * (lane & 3));
      const unsigned one = 1u;
      asm volatile("ds_add_rtn_u32 %0, %3, %4\n\tds_read_b128 %1, %5\n\tds_read_b128 %2, %5 offset:16\n\ts_waitcnt lgkmcnt(0)"
                   : "=&v"(arr), "=&v"(u), "=&v"(v) : "v"(ca), "v"(one), "v"(fa) : "memory");
    }
    if (__builtin_amdgcn_readfirstlane(arr) == 3) {
      const unsigned ov = cvtpk((u[0] + u[1]) + (u[2] + u[3]), (v[0] + v[1]) + (v[2] + v[3]));
      const rsrc_t ro = __builtin_amdgcn_make_buffer_rsrc((void*)p.out, 0, DM * 2, RSRC3);
      __builtin_amdgcn_raw_buffer_store_b128(u32x4{ov, dppu<0x55>(ov), dppu<0xAA>(ov), dppu<0xFF>(ov)}, ro, lane == 0 ? b * 16 : DM * 2, 0, AUX_SYS);
      if (b == 0 && lane == 0) __hip_atomic_store((GAS unsigned*)(p.ws + WS_G), tag, __ATOMIC_RELAXED, __HIP_MEMORY_SCOPE_AGENT);
      STAMP(11);
      STAMP_FLUSH(10, 12);
    }
  }
  if (w == 0) { STAMP_FLUSH(0, 1); STAMP_FLUSH(7, 9); }
  if (mine) STAMP_FLUSH(6, 7);
}

extern "C" void kernel_launch(void* const* d_in, const int* in_sizes, int n_in, void* d_out, int out_size, void* d_ws, size_t ws_size, hipStream_t stream) {
  Params p;
  p.x = (const u16*)d_in[0];
  p.kA = (const u16*)d_in[1]; p.vA = (const u16*)d_in[2]; p.wqA = (const u16*)d_in[3]; p.woA = (const u16*)d_in[4];
  p.kB = (const u16*)d_in[6]; p.vB = (const u16*)d_in[7]; p.wqB = (const u16*)d_in[8]; p.woB = (const u16*)d_in[9];
  p.gw = (const u16*)d_in[10];
  p.out = (u16*)d_out; p.ws = (unsigned char*)d_ws;
  k_fused<<<NWG, NTHR, 0, stream>>>(p);
}
```

```cpp
#include <hip/hip_runtime.h>
#include <hip/hip_bf16.h>
#include <stdint.h>

typedef unsigned u32x4 __attribute__((ext_vector_type(4)));
typedef unsigned u32x2 __attribute__((ext_vector_type(2)));
typedef float f32x4 __attribute__((ext_vector_type(4)));
typedef float f32x2 __attribute__((ext_vector_type(2)));
typedef __bf16 bf16x2 __attribute__((ext_vector_type(2)));
typedef unsigned short u16;
typedef unsigned long long u64;
typedef __amdgpu_buffer_rsrc_t rsrc_t;
#define GAS __attribute__((address_space(1)))
#define LAS __attribute__((address_space(3)))

constexpr int DM = 448, DH = 256, SK = 8192, NQ = 4096;
constexpr int NWG = 256, NTHR = 512, NFIN = 56;
constexpr int TAU = 106;
constexpr int TOFF = -4;
constexpr int GN = 4, GP = 5;
constexpr int D0 = 6, DMIN = 4, DMAX = 10;
constexpr int FOFF = 50;
constexpr int XA = 152, XB = 90;
constexpr int QDELAY = 48, EDELAY = 48, WOD = 5, DEL2 = 22, NBC = 32;
constexpr int WS_G = 0;
constexpr int WS_T0 = 1024;
constexpr int WS_QG = 4096;
constexpr int WS_OB1 = WS_QG + 131072;
constexpr int WS_IB2 = WS_OB1 + 8 * 96 * 1024;
constexpr int WS_END = WS_IB2 + NFIN * 8192;
constexpr unsigned SPIN_CAP = 1u << 18;
constexpr int AUX_SC1 = 16, AUX_NT = 2, AUX_SYS = 17, RSRC3 = 0x00020000;
constexpr int L_MRG = 0;
constexpr int L_ML = 12 * DH;
constexpr int L_QRED = L_ML + 32;
constexpr int L_Q = L_QRED + 256;
constexpr int L_CNT = L_Q + 512;
constexpr int L_PR = L_CNT + 4;
constexpr int L_O = L_PR + 4;
constexpr int L_FIN = L_O + 16;
constexpr int L_P = L_FIN + 32;
constexpr int L_END = L_P + 512;

__device__ __forceinline__ float dot2(unsigned a, unsigned b, float acc) {
  return __builtin_amdgcn_fdot2_f32_bf16(__builtin_bit_cast(bf16x2, a), __builtin_bit_cast(bf16x2, b), acc, false);
}
__device__ __forceinline__ float bflo(unsigned u) { return __builtin_bit_cast(float, u << 16); }
__device__ __forceinline__ float bfhi(unsigned u) { return __builtin_bit_cast(float, u & 0xffff0000u); }
__device__ __forceinline__ unsigned cvtpk(float lo, float hi) { return __builtin_bit_cast(unsigned, __builtin_convertvector(f32x2{lo, hi}, bf16x2)); }
__device__ __forceinline__ float vmax(float a, float b) { return __builtin_elementwise_maximum(a, b); }
template <int CTRL> __device__ __forceinline__ unsigned dppu(unsigned x) { return (unsigned)__builtin_amdgcn_mov_dpp((int)x, CTRL, 0xf, 0xf, true); }
template <int CTRL> __device__ __forceinline__ float dpp(float x) { return __uint_as_float(dppu<CTRL>(__float_as_uint(x))); }
constexpr int XOR1 = 0xB1, XOR2 = 0x4E, XOR7 = 0x141, XOR8 = 0x128, ROR4 = 0x124;
__device__ __forceinline__ float tr32(float a, float b) {
  auto t = __builtin_amdgcn_permlane32_swap(__float_as_uint(a), __float_as_uint(b), false, false);
  return __uint_as_float(t[0]) + __uint_as_float(t[1]);
}
__device__ __forceinline__ float tr16(float a, float b) {
  auto t = __builtin_amdgcn_permlane16_swap(__float_as_uint(a), __float_as_uint(b), false, false);
  return __uint_as_float(t[0]) + __uint_as_float(t[1]);
}
__device__ __forceinline__ float mx32(float a) {
  auto t = __builtin_amdgcn_permlane32_swap(__float_as_uint(a), __float_as_uint(a), false, false);
  return vmax(__uint_as_float(t[0]), __uint_as_float(t[1]));
}
__device__ __forceinline__ float mx16(float a) {
  auto t = __builtin_amdgcn_permlane16_swap(__float_as_uint(a), __float_as_uint(a), false, false);
  return vmax(__uint_as_float(t[0]), __uint_as_float(t[1]));
}
__device__ __forceinline__ float rowsum16(float x) { x += dpp<XOR1>(x); x += dpp<XOR2>(x); x += dpp<XOR7>(x); x += dpp<XOR8>(x); return x; }
__device__ __forceinline__ u32x4 sel4(bool c, u32x4 a, u32x4 b) { return u32x4{c ? a[0] : b[0], c ? a[1] : b[1], c ? a[2] : b[2], c ? a[3] : b[3]}; }
__device__ __forceinline__ unsigned now32() { return (unsigned)__builtin_amdgcn_s_memrealtime(); }
#define SCHED() __builtin_amdgcn_sched_barrier(0)
#define BARRIER() do { SCHED(); asm volatile("s_waitcnt lgkmcnt(0)" ::: "memory"); __builtin_amdgcn_s_barrier(); asm volatile("" ::: "memory"); SCHED(); } while (0)
#define VMWAIT(n) asm volatile("s_waitcnt vmcnt(%0)" :: "n"(n) : "memory")

template <int N, int SH, int SLP = 4>
__device__ __forceinline__ void gather(rsrc_t rs, int o0, int step, unsigned tagw, int few, u32x4 (&g)[N]) {
  unsigned bad = 0;
#pragma unroll
  for (int i = 0; i < N; ++i) g[i] = __builtin_amdgcn_raw_buffer_load_b128(rs, o0 + i * step, 0, AUX_SC1);
#pragma unroll
  for (int i = 0; i < N; ++i) bad |= (g[i][1] ^ tagw) | (g[i][3] ^ tagw);
  if (__builtin_amdgcn_ballot_w64((bad >> SH) != 0) == 0) return;
#pragma nounroll
  for (unsigned spins = 0; spins < SPIN_CAP; ++spins) {
    bool m[N];
    int left = 0;
#pragma unroll
    for (int i = 0; i < N; ++i) {
      m[i] = (((g[i][1] ^ tagw) | (g[i][3] ^ tagw)) >> SH) != 0;
      left += __builtin_popcountll(__builtin_amdgcn_ballot_w64(m[i]));
    }
    if (left == 0) break;
    if (left > few) __builtin_amdgcn_s_sleep(SLP);
    asm volatile("" ::: "memory");
    u32x4 a[N];
#pragma unroll
    for (int i = 0; i < N; ++i) a[i] = __builtin_amdgcn_raw_buffer_load_b128(rs, m[i] ? o0 + i * step : WS_END, 0, AUX_SC1);
#pragma unroll
    for (int i = 0; i < N; i += 2) asm volatile("" : "+v"(a[i]), "+v"(a[i + 1]));
#pragma unroll
    for (int i = 0; i < N; ++i) g[i] = sel4(m[i], a[i], g[i]);
  }
}

template <int LO, int HI>
__device__ __forceinline__ void gate_rt(int d) {
  asm volatile(
      "s_waitcnt vmcnt(%1)\n\t"
      "s_cmp_gt_i32 %0, %2\n\ts_cbranch_scc1 .Lg%=\n\t"
      "s_waitcnt vmcnt(%3)\n\t"
      "s_cmp_gt_i32 %0, %3\n\ts_cbranch_scc1 .Lg%=\n\t"
      "s_waitcnt vmcnt(%4)\n\t"
      "s_cmp_gt_i32 %0, %4\n\ts_cbranch_scc1 .Lg%=\n\t"
      "s_waitcnt vmcnt(%5)\n\t"
      "s_cmp_gt_i32 %0, %5\n\ts_cbranch_scc1 .Lg%=\n\t"
      "s_waitcnt vmcnt(%6)\n\t"
      ".Lg%=:" :: "s"(d), "n"(HI - 1), "n"(LO + 3), "n"(LO + 2), "n"(LO + 1), "n"(LO), "n"(LO - 1) : "scc", "memory");
}

struct Params {
  const u16 *x, *kA, *vA, *wqA, *woA, *kB, *vB, *wqB, *woB, *gw;
  u16* out;
  unsigned char* ws;
};

enum { NONE, FIXED, PACED, LAST };
typedef int i32x4 __attribute__((ext_vector_type(4)));
struct Next { rsrc_t r; i32x4 voff; int so0, so1, dg; };
template <int MODE>
__device__ __forceinline__ void refill(u32x4 (&d)[8], int i, const Next& nx) {
  if (MODE == FIXED) VMWAIT(D0);
  if (MODE == PACED) gate_rt<DMIN, DMAX>(nx.dg);
  if (MODE == LAST) d[i] = __builtin_amdgcn_raw_buffer_load_b128(nx.r, nx.voff[0], i * 4 * DM * 2, AUX_NT);
  else if (MODE != NONE) d[i] = __builtin_amdgcn_raw_buffer_load_b128(nx.r, nx.voff[i & 3], i < 4 ? nx.so0 : nx.so1, AUX_NT);
  SCHED();
}
template <int NH, int MODE, bool TWO = false, int NRF = 8>
__device__ __forceinline__ void k_step(u32x4 (&d)[8], const u32x4 (&e)[8], const u32x4 (&qv)[2], float (&mx)[2], float* sp, int lane, const Next& nx) {
  constexpr float C2 = 0.0625f * 1.4426950408889634f;
  constexpr int NU = TWO ? 2 * NH : NH;
  float sc[NU][8];
#pragma unroll
  for (int i = 0; i < 8; ++i) {
#pragma unroll
    for (int u = 0; u < NU; ++u) {
      const u32x4 kk = u < NH ? d[i] : e[i], q = qv[u % NH];
      float t = dot2(kk[0], q[0], 0.f); t = dot2(kk[1], q[1], t); t = dot2(kk[2], q[2], t); sc[u][i] = dot2(kk[3], q[3], t);
      if (i & 1) sc[u][i >> 1] = tr16(sc[u][i - 1], sc[u][i]);
      asm volatile("" : "+v"(sc[u][(i & 1) ? i >> 1 : i]));
    }
    if (i < NRF) refill<MODE>(d, i, nx);
  }
#pragma unroll
  for (int u = 0; u < NU; ++u) {
    const float a0 = sc[u][0] + dpp<XOR8>(sc[u][0]), a1 = sc[u][1] + dpp<XOR8>(sc[u][1]), a2 = sc[u][2] + dpp<XOR8>(sc[u][2]), a3 = sc[u][3] + dpp<XOR8>(sc[u][3]);
    const float b0 = (lane & 8) ? a2 : a0, b1 = (lane & 8) ? a3 : a1;
    const float c0 = b0 + dpp<XOR7>(b0), c1 = b1 + dpp<XOR7>(b1);
    float t = (lane & 4) ? c1 : c0;
    t += dpp<XOR2>(t); t += dpp<XOR1>(t);
    t *= C2;
    mx[u % NH] = vmax(mx[u % NH], t);
    sp[(u % NH) * 64 + (u / NH) * 8] = t;
  }
}
template <int NH, int MODE>
__device__ __forceinline__ void v_step(u32x4 (&d)[8], f32x2 (&acc)[2][4], const float* pp, const Next& nx) {
  f32x4 pr[NH][2];
#pragma unroll
  for (int h = 0; h < NH; ++h) { pr[h][0] = *(const f32x4*)(pp + h * 64); pr[h][1] = *(const f32x4*)(pp + h * 64 + 4); }
#pragma unroll
  for (int i = 0; i < 8; ++i) {
    f32x2 vf[4];
#pragma unroll
    for (int k = 0; k < 4; ++k) vf[k] = f32x2{bflo(d[i][k]), bfhi(d[i][k])};
#pragma unroll
    for (int h = 0; h < NH; ++h) {
      const f32x2 p2 = f32x2{pr[h][i >> 2][i & 3], pr[h][i >> 2][i & 3]};
#pragma unroll
      for (int k = 0; k < 4; ++k) acc[h][k] = __builtin_elementwise_fma(p2, vf[k], acc[h][k]);
      asm volatile("" : "+v"(acc[h][0]), "+v"(acc[h][1]), "+v"(acc[h][2]), "+v"(acc[h][3]));
    }
    refill<MODE>(d, i, nx);
  }
}
template <int NH>
__device__ __forceinline__ void softmax(float* pw, float* ml, float (&mx)[2], int lane) {
  float m[2], l[2];
#pragma unroll
  for (int h = 0; h < NH; ++h) { float t = mx[h]; t = vmax(t, dpp<XOR7>(t)); t = vmax(t, dpp<XOR8>(t)); t = mx16(t); m[h] = mx32(t); }
  asm volatile("s_waitcnt lgkmcnt(0)" ::: "memory");
#pragma unroll
  for (int u = 0; u < 2; ++u) { l[u] = __builtin_amdgcn_exp2f(pw[u * 64 + lane] - m[NH == 2 ? u : 0]); pw[u * 64 + lane] = l[u]; }
  if (NH == 1) l[0] += l[1];
#pragma unroll
  for (int h = 0; h < NH; ++h) { float t = rowsum16(l[h]); t = tr16(t, t); t = tr32(t, t); *(f32x2*)(ml + 8 * h) = f32x2{m[h], t}; }
  asm volatile("s_waitcnt lgkmcnt(0)" ::: "memory");
}
template <int NH>
__device__ __forceinline__ void flush(float* mrg, int lane, f32x2 (&acc)[2][4]) {
#pragma unroll
  for (int h = 0; h < NH; ++h) {
    f32x4 o;
    o[0] = tr32(acc[h][0][0], acc[h][2][0]); o[1] = tr32(acc[h][0][1], acc[h][2][1]);
    o[2] = tr32(acc[h][1][0], acc[h][3][0]); o[3] = tr32(acc[h][1][1], acc[h][3][1]);
    *(f32x4*)(mrg + 4 * h * DH + 8 * (lane & 31) + 4 * (lane >> 5)) = o;
#pragma unroll
    for (int k = 0; k < 4; ++k) acc[h][k] = f32x2{0.f, 0.f};
  }
}

__device__ __forceinline__ void stream(const Params& p, float* lds, int j, int idx, int w4, int lane) {
  const int oct = idx >> 3;
  const rsrc_t kB = __builtin_amdgcn_make_buffer_rsrc((void*)(p.kB + (size_t)(4 * j + oct) * (SK / 4) * DH), 0, SK / 4 * DH * 2, RSRC3);
  const rsrc_t vB = __builtin_amdgcn_make_buffer_rsrc((void*)(p.vB + (size_t)(4 * j + oct) * (SK / 4) * DH), 0, SK / 4 * DH * 2, RSRC3);
  const rsrc_t kA = __builtin_amdgcn_make_buffer_rsrc((void*)(p.kA + (size_t)(4 * j + oct) * (SK / 2) * DH), 0, SK / 2 * DH * 2, RSRC3);
  const rsrc_t vA = __builtin_amdgcn_make_buffer_rsrc((void*)(p.vA + (size_t)(4 * j + oct) * (SK / 2) * DH), 0, SK / 2 * DH * 2, RSRC3);
  const int rot = 3 * j + oct, inw = (idx & 7) * 16384 + w4 * 1024;
  auto coff = [=](int c) { return (((c & 8) | ((c + rot) & 7)) << 17) + inw; };
  i32x4 voff;
#pragma unroll
  for (int i = 0; i < 4; ++i) voff[i] = lane * 16 + ((i + idx) & 3) * 4096;
  u32x4 buf[4][8];
  int fd = 0; unsigned t0 = 0;
  BARRIER();
#pragma unroll
  for (int n = 0; n < 32; ++n) {
    if (n >= 8 && (n & 3) == 0) fd = D0 + (((int)(now32() - t0 - FOFF - n * TAU / 8) * 4) >> 8);
    if (n >= 8) gate_rt<D0 - 1, DMAX>(fd); else if (n >= D0) VMWAIT(D0 - 1);
    if (n == D0 - 1) { t0 = now32(); asm volatile("" : "+s"(t0)); }
    buf[n >> 3][n & 7] = __builtin_amdgcn_raw_buffer_load_b128(kB, voff[n & 3], coff(n >> 2), AUX_NT);
    SCHED();
  }
  BARRIER();
  unsigned ep = now32(), due = TOFF + TAU;
  auto next = [&](rsrc_t r, int c) {
    due += TAU + ((c >> 1) & 1);
    const int lag = (int)(now32() - ep - due);
    return Next{r, voff, coff(c), coff(c + 1), D0 + ((lag * (lag < 0 ? GN : GP)) >> 8)};
  };
  auto sync_epoch = [&]() { const unsigned e = __builtin_amdgcn_readfirstlane(*(volatile LAS unsigned*)(lds + L_CNT + 3)); ep = e ? e : ep; };
  auto report = [&](int rd) { if (w4 == 0) *(volatile LAS unsigned*)(lds + L_PR + rd) = (now32() - due - TAU) | 1u; };
  float* pw = lds + L_P + w4 * 128;
  const int li = ((lane >> 4) & 1) | ((lane >> 1) & 6), hf = lane >> 5;
  u32x4 qv[2];
  float mx[2] = {-INFINITY, -INFINITY};
  f32x2 acc[2][4];
#pragma unroll
  for (int h = 0; h < 2; ++h)
#pragma unroll
    for (int k = 0; k < 4; ++k) acc[h][k] = f32x2{0.f, 0.f};
  qv[0] = *(const u32x4*)(lds + L_Q + 2 * 128 + 4 * (lane & 31));
  qv[1] = *(const u32x4*)(lds + L_Q + 3 * 128 + 4 * (lane & 31));
  k_step<2, FIXED, true>(buf[0], buf[1], qv, mx, pw + hf * 32 + li, lane, Next{vB, voff, coff(0), coff(1), 0});
  { const Next nx = {vB, voff, coff(2), coff(3), 0};
    k_step<2, FIXED, true, 6>(buf[2], buf[3], qv, mx, pw + hf * 32 + li + 16, lane, nx);
    refill<FIXED>(buf[2], 6, nx);
    softmax<2>(pw, lds + L_ML + w4 * 2, mx, lane);
    refill<FIXED>(buf[2], 7, nx); }
#pragma nounroll
  for (int n = 0; n < 4; n += 2) {
    const rsrc_t r = n == 0 ? vB : kA; const int c = n == 0 ? 4 : 0;
    sync_epoch();
    v_step<2, PACED>(buf[0], acc, pw + hf * 32 + n * 8, next(r, c));
    v_step<2, PACED>(buf[2], acc, pw + hf * 32 + n * 8 + 8, next(r, c + 2));
  }
  flush<2>(lds + L_MRG + w4 * DH, lane, acc);
  qv[0] = *(const u32x4*)(lds + L_Q + (oct >> 1) * 128 + 4 * (lane & 31));
  mx[0] = -INFINITY;
#pragma nounroll
  for (int n = 0; n < 8; n += 2) {
    const rsrc_t r = n < 6 ? kA : vA; const int c = n < 6 ? 2 * n + 4 : 0;
    if (n == 2) report(1);
    sync_epoch();
    k_step<1, PACED>(buf[0], buf[0], qv, mx, pw + hf * 64 + li + n * 8, lane, next(r, c));
    k_step<1, PACED>(buf[2], buf[2], qv, mx, pw + hf * 64 + li + n * 8 + 8, lane, next(r, c + 2));
  }
  softmax<1>(pw, lds + L_ML + (8 + w4) * 2, mx, lane);
#pragma nounroll
  for (int n = 0; n < 6; n += 2) {
    if (n == 0) report(2);
    sync_epoch();
    v_step<1, PACED>(buf[0], acc, pw + hf * 64 + n * 8, next(vA, 2 * n + 4));
    v_step<1, PACED>(buf[2], acc, pw + hf * 64 + n * 8 + 8, next(vA, 2 * n + 6));
  }
  BARRIER();
  const bool late = oct >= 2;
  const u16* wop = (oct < 2 ? p.woA : p.woB) + (size_t)((2 * j + (oct & 1)) * DH + (idx & 7) * 32) * DM;
  const int cb = w4 * 256 + (lane & 15) * 16;
  Next nw = {__builtin_amdgcn_make_buffer_rsrc((void*)wop, 0, late ? 0 : 32 * DM * 2, RSRC3), i32x4{cb < DM * 2 ? (lane >> 4) * (DM * 2) + cb : 32 * DM * 2, 0, 0, 0}, 0, 0, 0};
  v_step<1, NONE>(buf[0], acc, pw + hf * 64 + 48, nw);
  v_step<1, LAST>(buf[2], acc, pw + hf * 64 + 56, nw);
  flush<1>(lds + L_MRG + (8 + w4) * DH, lane, acc);
  asm volatile("" ::: "memory");
  if (lane == 0) __hip_atomic_fetch_add((LAS unsigned*)(lds + L_CNT), 1u, __ATOMIC_RELAXED, __HIP_MEMORY_SCOPE_WORKGROUP);
  asm volatile("" ::: "memory");
  if (late) {
    nw.r = __builtin_amdgcn_make_buffer_rsrc((void*)wop, 0, 32 * DM * 2, RSRC3);
    __builtin_amdgcn_s_sleep(WOD);
    for (int i = 0; i < 256 && __builtin_amdgcn_readfirstlane(*(volatile LAS unsigned*)(lds + L_PR)) == 0; ++i) __builtin_amdgcn_s_sleep(1);
#pragma unroll
    for (int i = 0; i < 8; ++i) refill<LAST>(buf[2], i, nw);
  }
  const unsigned tag = __builtin_amdgcn_readfirstlane(*(LAS unsigned*)(lds + L_CNT + 2));
  const rsrc_t rs = __builtin_amdgcn_make_buffer_rsrc((void*)p.ws, 0, WS_END, RSRC3);
  int yo = (cb < DM * 2 && (lane & 16) == 0) ? WS_IB2 + (cb >> 4) * 8192 + blockIdx.x * 32 + hf * 16 : WS_END;
  unsigned wp[4][8];
#pragma unroll
  for (int k = 0; k < 4; ++k)
#pragma unroll
    for (int c = 0; c < 4; ++c) {
      const u32x4 (&wt)[8] = buf[2];
      wp[k][2 * c] = __builtin_amdgcn_perm(wt[2 * k + 1][c], wt[2 * k][c], 0x05040100u); wp[k][2 * c + 1] = __builtin_amdgcn_perm(wt[2 * k + 1][c], wt[2 * k][c], 0x07060302u);
    }
#pragma unroll
  for (int k = 0; k < 4; ++k) asm volatile("" : "+v"(wp[k][0]), "+v"(wp[k][1]), "+v"(wp[k][2]), "+v"(wp[k][3]), "+v"(wp[k][4]), "+v"(wp[k][5]), "+v"(wp[k][6]), "+v"(wp[k][7]));
  asm volatile("" : "+v"(yo));
  BARRIER();
  const u32x4 ov = *(const u32x4*)(lds + L_O + 4 * (lane >> 4));
  float y[8];
#pragma unroll
  for (int c = 0; c < 8; ++c) y[c] = dot2(wp[3][c], ov[3], dot2(wp[2][c], ov[2], dot2(wp[1][c], ov[1], dot2(wp[0][c], ov[0], 0.f))));
#pragma unroll
  for (int c = 0; c < 4; ++c) y[c] = tr32(y[c], y[c + 4]);
  const float z0 = tr16(y[0], y[2]), z1 = tr16(y[1], y[3]);
  const unsigned yp = cvtpk(z0, z1);
  const auto ys = __builtin_amdgcn_permlane16_swap(yp, yp, false, false);
  __builtin_amdgcn_raw_buffer_store_b128(u32x4{ys[0], tag, ys[1], tag}, rs, yo, 0, AUX_SC1);
  asm volatile(".Lkf_end:");
}

struct Mrg { f32x2 e0, e1, e2, e3; unsigned tw1, tw3; };
__device__ __forceinline__ Mrg merge_weights(const float* ml, unsigned tag, int lane) {
  const f32x4 ma = *(const f32x4*)ml, mb = *(const f32x4*)(ml + 4);
  const float M = vmax(vmax(ma[0], ma[2]), vmax(mb[0], mb[2]));
  const float a0 = __builtin_amdgcn_exp2f(ma[0] - M), a1 = __builtin_amdgcn_exp2f(ma[2] - M), a2 = __builtin_amdgcn_exp2f(mb[0] - M), a3 = __builtin_amdgcn_exp2f(mb[2] - M);
  const float L = a0 * ma[1] + a1 * ma[3] + a2 * mb[1] + a3 * mb[3];
  const float rl = __builtin_amdgcn_rcpf(L);
  const unsigned lam = __float_as_uint(M + __builtin_amdgcn_logf(L)) >> ((lane & 1) * 16);
  return Mrg{f32x2{a0 * rl, a0 * rl}, f32x2{a1 * rl, a1 * rl}, f32x2{a2 * rl, a2 * rl}, f32x2{a3 * rl, a3 * rl}, (tag << 8) | (lam & 0xffu), (tag << 8) | ((lam >> 8) & 0xffu)};
}
__device__ __forceinline__ void publish(const float* mp, const float* cnt, unsigned want, const Mrg& m, rsrc_t rs, int off) {
  f32x4 c0, c1, c2, c3;
  const unsigned ca = (unsigned)(size_t)(const LAS float*)cnt, pa = (unsigned)(size_t)(const LAS float*)mp;
#pragma nounroll
  for (unsigned spins = 0; spins < (SPIN_CAP << 4); ++spins) {
    unsigned c;
    asm volatile("ds_read_b32 %0, %5\n\tds_read_b128 %1, %6\n\tds_read_b128 %2, %6 offset:1024\n\tds_read_b128 %3, %6 offset:2048\n\tds_read_b128 %4, %6 offset:3072\n\ts_waitcnt lgkmcnt(0)"
                 : "=&v"(c), "=&v"(c0), "=&v"(c1), "=&v"(c2), "=&v"(c3) : "v"(ca), "v"(pa) : "memory");
    if (__builtin_amdgcn_readfirstlane(c) >= want) break;
  }
  const f32x2 lo = (m.e0 * c0.xy + m.e1 * c1.xy) + (m.e2 * c2.xy + m.e3 * c3.xy), hi = (m.e0 * c0.zw + m.e1 * c1.zw) + (m.e2 * c2.zw + m.e3 * c3.zw);
  __builtin_amdgcn_raw_buffer_store_b128(u32x4{cvtpk(lo[0], lo[1]), m.tw1, cvtpk(hi[0], hi[1]), m.tw3}, rs, off, 0, AUX_SC1);
  SCHED();
}
template <int N>
__device__ __forceinline__ void combine(float* lds, rsrc_t rs, int ob, unsigned tag, float gate, int lane) {
  u32x4 g[N];
  asm volatile("" ::: "memory");
  gather<N, 8, 10>(rs, ob, 8192, tag << 8, 24, g);
  float la[N], M = -INFINITY;
#pragma unroll
  for (int i = 0; i < N; ++i) {
    unsigned lb = __builtin_amdgcn_perm(g[i][3], g[i][1], 0x0c0c0400u) << ((lane & 1) * 16);
    lb |= dppu<XOR1>(lb);
    la[i] = __uint_as_float(lb); M = i ? vmax(M, la[i]) : la[i];
  }
  M = vmax(M, dpp<XOR8>(M)); M = mx16(M); M = mx32(M);
  float L = 0.f, o0 = 0.f, o1 = 0.f, o2 = 0.f, o3 = 0.f;
#pragma unroll
  for (int i = 0; i < N; ++i) {
    const float f = __builtin_amdgcn_exp2f(la[i] - M);
    L += f; o0 = fmaf(f, bflo(g[i][0]), o0); o1 = fmaf(f, bfhi(g[i][0]), o1); o2 = fmaf(f, bflo(g[i][2]), o2); o3 = fmaf(f, bfhi(g[i][2]), o3);
  }
  float r = tr16(tr32(o0, o2), tr32(o1, o3));
  L = tr32(L, L); L = tr16(L, L);
  r += dpp<XOR8>(r); L += dpp<XOR8>(L);
  r *= gate * __builtin_amdgcn_rcpf(L);
  ((LAS u16*)(lds + L_O))[(lane >> 4) * 8 + (lane & 7)] = (u16)cvtpk(r, r);
}

__global__ void __launch_bounds__(NTHR, 2) k_fused(Params p) {
  __shared__ __attribute__((aligned(16))) float lds[L_END];
  const int b = blockIdx.x, j = b & 7, idx = b >> 3;
  const int tid = threadIdx.x, lane = tid & 63, w = __builtin_amdgcn_readfirstlane(tid >> 6);
  u64 pc; unsigned clen;
  asm volatile("s_getpc_b64 %0\n.Lkf_beg:\n\ts_mov_b32 %1, .Lkf_end-.Lkf_beg" : "=s"(pc), "=s"(clen));
  u32x4 cpf = {0u, 0u, 0u, 0u};
  if (w == 3) cpf = __builtin_amdgcn_raw_buffer_load_b128(__builtin_amdgcn_make_buffer_rsrc((void*)(pc & ~15ull), 0, clen, RSRC3), idx * 1024 + lane * 16, 0, 0);
#define ARGS() asm volatile("" :: "s"(p.x), "s"(p.kA), "s"(p.vA), "s"(p.wqA), "s"(p.woA), "s"(p.kB), "s"(p.vB), "s"(p.wqB), "s"(p.woB), "s"(p.gw), "s"(p.out), "s"(p.ws))
  if (w >= 4) {
    ARGS();
    stream(p, lds, j, idx, w - 4, lane);
    return;
  }
  const rsrc_t rs = __builtin_amdgcn_make_buffer_rsrc((void*)p.ws, 0, WS_END, RSRC3);
  const int myqh = idx >> 3, sl = idx & 7, hd = 2 * j + (myqh & 1);
  const bool mine = w >= 2 || w == (idx >> 4);
  unsigned tag;
  float gate;
  {
    const int r0 = tid < 224 ? (sl & 1) * 224 + (tid >> 3) * 8 : DM, c8 = tid & 7;
    int wqo = (r0 * NQ + hd * DH + (sl >> 1) * 64 + 8 * c8) * 2, xo = r0 * 2;
    asm volatile("" : "+v"(wqo), "+v"(xo));
    ARGS();
    const rsrc_t rq = __builtin_amdgcn_make_buffer_rsrc((void*)(myqh < 2 ? p.wqA : p.wqB), 0, DM * NQ * 2, RSRC3);
    u32x4 wv[8];
#pragma unroll
    for (int i = 0; i < 8; ++i) wv[i] = __builtin_amdgcn_raw_buffer_load_b128(rq, wqo, i * NQ * 2, AUX_NT);
    const rsrc_t rx = __builtin_amdgcn_make_buffer_rsrc((void*)p.x, 0, DM * 2, RSRC3);
    const u32x4 xv = __builtin_amdgcn_raw_buffer_load_b128(rx, xo, 0, 0);
    const unsigned G = __hip_atomic_load((GAS unsigned*)(p.ws + WS_G), __ATOMIC_RELAXED, __HIP_MEMORY_SCOPE_AGENT);
    const u32x4 xa = __builtin_amdgcn_raw_buffer_load_b128(rx, tid * 16, 0, 0), ga = __builtin_amdgcn_raw_buffer_load_b128(__builtin_amdgcn_make_buffer_rsrc((void*)p.gw, 0, DM * 2, RSRC3), tid * 16, 0, 0);
    if (tid < 2) *(LAS unsigned*)(lds + L_CNT + tid) = 0u;
    BARRIER();
    float a[8];
#pragma unroll
    for (int k = 0; k < 4; ++k) {
      float lo = 0.f, hi = 0.f;
#pragma unroll
      for (int i = 0; i < 4; ++i) {
        lo = dot2(__builtin_amdgcn_perm(wv[2 * i + 1][k], wv[2 * i][k], 0x05040100u), xv[i], lo);
        hi = dot2(__builtin_amdgcn_perm(wv[2 * i + 1][k], wv[2 * i][k], 0x07060302u), xv[i], hi);
      }
      a[2 * k] = lo; a[2 * k + 1] = hi;
    }
#pragma unroll
    for (int k = 0; k < 4; ++k) a[k] = tr32(a[k], a[k + 4]);
#pragma unroll
    for (int k = 0; k < 2; ++k) a[k] = tr16(a[k], a[k + 2]);
#pragma unroll
    for (int k = 0; k < 2; ++k) a[k] += dpp<XOR8>(a[k]);
    if ((lane & 8) == 0) *(f32x2*)(lds + L_QRED + w * 64 + 8 * c8 + 4 * (lane >> 5) + 2 * ((lane >> 4) & 1)) = f32x2{a[0], a[1]};
    tag = __builtin_amdgcn_readfirstlane(G) + 1u;
    if (tid == 0) *(LAS unsigned*)(lds + L_CNT + 2) = tag;
    asm volatile("s_waitcnt lgkmcnt(0)" :: "s"(tag) : "memory");
    unsigned arr = 0;
    if (lane == 0) arr = __hip_atomic_fetch_add((LAS unsigned*)(lds + L_CNT), 1u, __ATOMIC_RELAXED, __HIP_MEMORY_SCOPE_WORKGROUP);
    arr = __builtin_amdgcn_readfirstlane(arr);
    asm volatile("" ::: "memory");
    if (arr == 3 && lane < 32) {
      const float* qr = lds + L_QRED + 2 * lane;
      const f32x2 sq = (*(const f32x2*)qr + *(const f32x2*)(qr + 64)) + (*(const f32x2*)(qr + 128) + *(const f32x2*)(qr + 192));
      __builtin_amdgcn_raw_buffer_store_b128(u32x4{__float_as_uint(sq[0]), tag, __float_as_uint(sq[1]), tag}, rs, WS_QG + j * 16384 + idx * 512 + lane * 16, 0, AUX_SC1);
    }
    asm volatile("" :: "v"(cpf));
    float z = dot2(xa[0], ga[0], 0.f); z = dot2(xa[1], ga[1], z); z = dot2(xa[2], ga[2], z); z = dot2(xa[3], ga[3], z);
    z = rowsum16(z); z = tr16(z, z); z = tr32(z, z);
    gate = 1.f / (1.f + __expf(myqh < 2 ? -z : z));
    asm volatile("" : "+v"(gate));
    __builtin_amdgcn_s_sleep(QDELAY);
    asm volatile("" ::: "memory");
    if (mine) {
      u32x4 g[4];
      gather<4, 0>(rs, WS_QG + j * 16384 + w * 4096 + lane * 16, 1024, tag, 0, g);
#pragma unroll
      for (int u = 0; u < 4; u += 2)
        lds[L_Q + w * 128 + u * 32 + lane] = __uint_as_float(cvtpk(tr32(__uint_as_float(g[u][0]), __uint_as_float(g[u + 1][0])), tr32(__uint_as_float(g[u][2]), __uint_as_float(g[u + 1][2]))));
    } else if (lane == 0) { *(LAS unsigned*)(lds + L_CNT + 3) = 0u; *(LAS u32x4*)(lds + L_PR) = u32x4{0u, 0u, 0u, 0u}; }
    BARRIER();
    if (!mine) {
#pragma nounroll
      for (int rd = 0; rd < 3; ++rd) {
        unsigned t1 = now32();
        if (rd) {
          for (int i = 0; i < 256; ++i) { t1 = __builtin_amdgcn_readfirstlane(*(volatile LAS unsigned*)(lds + L_PR + rd)); if (t1) break; __builtin_amdgcn_s_sleep(4); }
          if (!t1) break;
        }
        const int bo = WS_T0 + rd * 512;
        __builtin_amdgcn_raw_buffer_store_b128(u32x4{t1, tag, t1, tag}, rs, (lane == 0 && (idx & (256 / NBC - 1)) == 0) ? bo + ((idx / (256 / NBC)) * 8 + j) * 16 : WS_END, 0, AUX_SC1);
#pragma nounroll
        for (int it = 0; it < (rd ? 2 : 3); ++it) {
          __builtin_amdgcn_s_sleep(EDELAY);
          asm volatile("" ::: "memory");
          const u32x4 e = __builtin_amdgcn_raw_buffer_load_b128(rs, lane < NBC ? bo + lane * 16 : WS_END, 0, AUX_SC1);
          const bool ok = lane < NBC && e[1] == tag && e[3] == tag;
          const int cnt = __builtin_popcountll(__builtin_amdgcn_ballot_w64(ok));
          const float d = (float)(int)(e[0] - t1), rc = cnt ? 1.f / (float)cnt : 0.f;
          float mean = rowsum16(ok ? d : 0.f); mean = tr16(mean, mean) * rc;
          float mad = rowsum16(ok ? fabsf(d - mean) : 0.f); mad = tr16(mad, mad) * rc;
          if (lane == 0 && (rd == 0 || cnt >= NBC - 2)) *(volatile LAS unsigned*)(lds + L_CNT + 3) = (t1 + (int)(mean + (rd ? 0.4f : 0.8f) * mad) - (rd ? 4 : 0)) | 1u;
          if (cnt >= NBC - 2) break;
        }
      }
    }
  }
  const int obg = WS_OB1 + j * 96 * 1024;
  BARRIER();
  Mrg ma = merge_weights(lds + L_ML + (w >= 2 ? w - 2 : 2) * 8, tag, lane);
  int pof = obg + (w >= 2 ? 32 + (w - 2) * 32 + idx : idx) * 1024 + lane * 16;
  asm volatile("" : "+v"(ma.e0), "+v"(ma.e1), "+v"(ma.e2), "+v"(ma.e3), "+v"(ma.tw1), "+v"(ma.tw3), "+v"(pof));
  if (w >= 1) {
    publish(lds + L_MRG + (w >= 2 ? w - 2 : 2) * 4 * DH + 4 * lane, lds + L_CNT, w >= 2 ? 0u : 8u, ma, rs, pof);
  } else {
    const unsigned tdue = __builtin_amdgcn_readfirstlane(*(volatile LAS unsigned*)(lds + L_CNT + 3)) + TOFF + 20 * TAU + 9;
    const int lag = (int)(now32() - tdue);
    const int x = myqh < 2 ? XA : XB, own = lag + x - (lag > 80 ? 18 : 70);
    for (int i = 0; i < 128 && (int)(now32() - tdue) < (own > x ? own : x); ++i) __builtin_amdgcn_s_sleep(1);
    if (lane == 0) *(volatile LAS unsigned*)(lds + L_PR) = 1u;
    const int ob = obg + (lane >> 3) * 1024 + sl * 128 + (lane & 7) * 16;
    if (myqh < 2) combine<2>(lds, rs, ob + myqh * 16 * 1024, tag, gate, lane);
    else combine<4>(lds, rs, ob + (myqh - 1) * 32 * 1024, tag, gate, lane);
  }
  BARRIER();
  if (b < NFIN) {
    const int ob = WS_IB2 + b * 8192 + w * 2048 + lane * 16;
    u32x4 g[2];
    __builtin_amdgcn_s_sleep(DEL2);
    asm volatile("" ::: "memory");
    gather<2, 0>(rs, ob, 1024, tag, 16, g);
    float r0 = tr32(bflo(g[0][0]) + bflo(g[1][0]), bflo(g[0][2]) + bflo(g[1][2])), r1 = tr32(bfhi(g[0][0]) + bfhi(g[1][0]), bfhi(g[0][2]) + bfhi(g[1][2]));
    r0 = tr16(r0, r1);
    r0 += dpp<XOR8>(r0); r0 += dpp<ROR4>(r0); r0 += dpp<XOR2>(r0);
    *(volatile LAS float*)(lds + L_FIN + (4 * (lane & 1) + 2 * (lane >> 5) + ((lane >> 4) & 1)) * 4 + w) = r0;
    unsigned arr; f32x4 u, v;
    {
      const unsigned ca = (unsigned)(size_t)(LAS float*)(lds + (lane == 0 ? L_CNT + 1 : L_QRED + lane)), fa = (unsigned)(size_t)(LAS float*)(lds + L_FIN + 8 * (lane & 3));
      const unsigned one = 1u;
      asm volatile("ds_add_rtn_u32 %0, %3, %4\n\tds_read_b128 %1, %5\n\tds_read_b128 %2, %5 offset:16\n\ts_waitcnt lgkmcnt(0)"
                   : "=&v"(arr), "=&v"(u), "=&v"(v) : "v"(ca), "v"(one), "v"(fa) : "memory");
    }
    if (__builtin_amdgcn_readfirstlane(arr) == 3) {
      const unsigned ov = cvtpk((u[0] + u[1]) + (u[2] + u[3]), (v[0] + v[1]) + (v[2] + v[3]));
      const rsrc_t ro = __builtin_amdgcn_make_buffer_rsrc((void*)p.out, 0, DM * 2, RSRC3);
      __builtin_amdgcn_raw_buffer_store_b128(u32x4{ov, dppu<0x55>(ov), dppu<0xAA>(ov), dppu<0xFF>(ov)}, ro, lane == 0 ? b * 16 : DM * 2, 0, AUX_SYS);
      if (b == 0 && lane == 0) __hip_atomic_store((GAS unsigned*)(p.ws + WS_G), tag, __ATOMIC_RELAXED, __HIP_MEMORY_SCOPE_AGENT);
    }
  }
}

extern "C" void kernel_launch(void* const* d_in, const int* in_sizes, int n_in, void* d_out, int out_size, void* d_ws, size_t ws_size, hipStream_t stream) {
  Params p;
  p.x = (const u16*)d_in[0];
  p.kA = (const u16*)d_in[1]; p.vA = (const u16*)d_in[2]; p.wqA = (const u16*)d_in[3]; p.woA = (const u16*)d_in[4];
  p.kB = (const u16*)d_in[6]; p.vB = (const u16*)d_in[7]; p.wqB = (const u16*)d_in[8]; p.woB = (const u16*)d_in[9];
  p.gw = (const u16*)d_in[10];
  p.out = (u16*)d_out; p.ws = (unsigned char*)d_ws;
  k_fused<<<NWG, NTHR, 0, stream>>>(p);
}
```
